```python
import torch

def _softmax(x, axis=-1):
    return torch.softmax(x, dim=axis)
def _gelu(x):
    return torch.nn.functional.gelu(x, approximate="tanh")
def _silu(x):
    return torch.nn.functional.silu(x)
def _sigmoid(x):
    return torch.sigmoid(x)
def _layernorm(x, axis=-1, eps=1e-5):
    m = x.mean(dim=axis, keepdim=True)
    v = x.var(dim=axis, keepdim=True, unbiased=False)
    return (x - m) / torch.sqrt(v + eps)
def _scatter_add(tgt, idx, src, axis=0):
    return tgt.clone().index_add_(axis, idx, src)
def _segment_sum(src, seg, n):
    out = torch.zeros((n,) + src.shape[1:], dtype=src.dtype)
    return out.index_add_(0, seg, src)
def _pad(x, pw):
    flat = tuple(v for pair in reversed(pw) for v in pair)
    return torch.nn.functional.pad(x, flat)

kernel_name = 'gated_mix__ssm_conv_s4__glu_gated_bilinear__prompt'

def setup_inputs():

    _w = (lambda a: (lambda t: t.to(torch.bfloat16) if t.is_floating_point() else t)(torch.as_tensor(a)))
    def _A_setup():
        B, L, D, N = 1, 2048, 576, 128
        A_re = -(lambda _x: torch.exp(torch.as_tensor(_x)))((lambda *s: torch.randn(*s, dtype=torch.bfloat16) * 3.2)(D, N))
        A_im = torch.pi * (lambda *s: torch.randn(*s, dtype=torch.bfloat16) * 0.05)(D, N)
        B_re = (lambda *s: torch.randn(*s, dtype=torch.bfloat16) * 0.1)(D, N)
        B_im = (lambda *s: torch.randn(*s, dtype=torch.bfloat16) * 0.1)(D, N)
        C_re = (lambda *s: torch.randn(*s, dtype=torch.bfloat16) * 0.1)(D, N)
        C_im = (lambda *s: torch.randn(*s, dtype=torch.bfloat16) * 0.1)(D, N)
        log_dt = (lambda *s: torch.randn(*s, dtype=torch.bfloat16) * 0.05)(D) * 0.5 - 4.0
        Dskip = (lambda *s: torch.randn(*s, dtype=torch.bfloat16) * 0.05)(D)
        return {
            "u": _w((lambda *s: torch.randn(*s, dtype=torch.bfloat16) * 0.05)(B, L, D)),
            "A_re": _w(A_re),
            "A_im": _w(A_im),
            "B_re": _w(B_re),
            "B_im": _w(B_im),
            "C_re": _w(C_re),
            "C_im": _w(C_im),
            "log_dt": _w(log_dt),
            "Dskip": _w(Dskip),
        }
    _A_in = _A_setup()

    def _B_setup():
        B, S, D, F = 1, 2048, 576, 4608
        return {
            "x": _w((lambda *s: torch.randn(*s, dtype=torch.bfloat16) * 0.05)(B, S, D)),
            "w_gate": _w((lambda *s: torch.randn(*s, dtype=torch.bfloat16) * 0.8)(D, F) * (D ** -0.5)),
            "w_up": _w((lambda *s: torch.randn(*s, dtype=torch.bfloat16) * 0.8)(D, F) * (D ** -0.5)),
            "w_down": _w((lambda *s: torch.randn(*s, dtype=torch.bfloat16) * 0.8)(F, D) * (F ** -0.5)),
        }
    _B_in = _B_setup()

    return {
        "A_u": _A_in["u"],
        "A_A_re": _A_in["A_re"],
        "A_A_im": _A_in["A_im"],
        "A_B_re": _A_in["B_re"],
        "A_B_im": _A_in["B_im"],
        "A_C_re": _A_in["C_re"],
        "A_C_im": _A_in["C_im"],
        "A_log_dt": _A_in["log_dt"],
        "A_Dskip": _A_in["Dskip"],
        "B_x": _B_in["x"],
        "B_w_gate": _B_in["w_gate"],
        "B_w_up": _B_in["w_up"],
        "B_w_down": _B_in["w_down"],
        "gate_w": _w((lambda *s: torch.randn(*s, dtype=torch.bfloat16) * 0.05)(576) * 16.0),
    }

def reference(A_u, A_A_re, A_A_im, A_B_re, A_B_im, A_C_re, A_C_im, A_log_dt, A_Dskip, B_x, B_w_gate, B_w_up, B_w_down, gate_w):
    def _A_ref(u, A_re, A_im, B_re, B_im, C_re, C_im, log_dt, Dskip):
        Bsz, L, D = u.shape
        dt = torch.exp(log_dt)[:, None]
        A = A_re + 1j * A_im
        Bc = B_re + 1j * B_im
        Cc = C_re + 1j * C_im
        logdA = dt * A
        dB = dt * Bc
        l = (lambda x, dt: x.to(dt))(torch.arange(L), A_re.dtype)
        P = torch.exp(logdA[:, :, None] * l[None, None, :])
        K = torch.einsum("dn,dnl,dn->dl", Cc, P, dB).real
        L2 = 2 * L
        Kp = _pad(K, ((0, 0), (0, L)))
        up = (lambda x, *d: x.permute(*d))(u, 0, 2, 1)
        up = _pad(up, ((0, 0), (0, 0), (0, L)))
        Kf = (lambda x, axes=(-2,-1): torch.fft.rfft2(x.float(), dim=axes))(Kp[:, None, :], axes=(-2, -1))
        Uf = (lambda x, axes=(-2,-1): torch.fft.rfft2(x.float(), dim=axes))(up[:, :, None, :], axes=(-2, -1))
        Yf = Uf * Kf[None, :, :, :]
        y = (lambda x, s, axes=(-2,-1): torch.fft.irfft2(x, s=s, dim=axes).to(torch.bfloat16))(Yf, (1, L2), axes=(-2, -1))[:, :, 0, :L]
        y = (lambda x, *d: x.permute(*d))(y, 0, 2, 1) + u * Dskip[None, None, :]
        return y

    def _B_ref(x, w_gate, w_up, w_down):
        g = torch.einsum("bsd,df->bsf", x, w_gate)
        u = torch.einsum("bsd,df->bsf", x, w_up)
        h = g * u
        return torch.einsum("bsf,fd->bsd", h, w_down)

    x = A_u
    g = _sigmoid((x * gate_w).sum(-1, keepdims=True))
    a = _A_ref(x, A_A_re, A_A_im, A_B_re, A_B_im, A_C_re, A_C_im, A_log_dt, A_Dskip)
    b = _B_ref(x, B_w_gate, B_w_up, B_w_down)
    return g * a + (1 - g) * b
```

```cpp
#include <hip/hip_runtime.h>
#include <hip/hip_bf16.h>
#include <stdint.h>

typedef unsigned short u16;
static __device__ __forceinline__ float bf2f(u16 v){ return __uint_as_float(((unsigned)v)<<16); }
static __device__ __forceinline__ u16 f2bf(float f){ __hip_bfloat16 b = __float2bfloat16(f); return *reinterpret_cast<u16*>(&b); }
static __device__ __forceinline__ float rbf(float f){ return bf2f(f2bf(f)); }

constexpr int L=2048, D=576, NS=128, F=4608;

__global__ void k_gate(const u16* x, const u16* gw, float* g){
  int s = blockIdx.x; int t = threadIdx.x;
  float acc=0; for(int d=t; d<D; d+=64) acc += rbf(bf2f(x[s*D+d])*bf2f(gw[d]));
  for(int o=32;o>0;o>>=1) acc += __shfl_xor(acc,o);
  if(t==0){ float z=rbf(acc); g[s]=rbf(1.f/(1.f+expf(-z))); }
}
__global__ void k_kgen(const u16* Are,const u16* Aim,const u16* Bre,const u16* Bim,const u16* Cre,const u16* Cim,const u16* ldt,float* K){
  int d=blockIdx.x;
  float dt=rbf(expf(bf2f(ldt[d])));
  for(int l=threadIdx.x;l<L;l+=blockDim.x){
    float lf=rbf((float)l);
    float acc=0;
    for(int n=0;n<NS;n++){
      float zr=dt*bf2f(Are[d*NS+n]), zi=dt*bf2f(Aim[d*NS+n]);
      float br=dt*bf2f(Bre[d*NS+n]), bi=dt*bf2f(Bim[d*NS+n]);
      float cr=bf2f(Cre[d*NS+n]), ci=bf2f(Cim[d*NS+n]);
      float wr=cr*br-ci*bi, wi=cr*bi+ci*br;
      float e=expf(zr*lf); float s,c; sincosf(zi*lf,&s,&c);
      acc += e*(wr*c-wi*s);
    }
    K[d*L+l]=acc;
  }
}
__global__ void k_conv(const u16* u,const float* K,const u16* Dsk,float* a){
  int t=blockIdx.x;
  for(int d=threadIdx.x; d<D; d+=blockDim.x){
    float acc=0; for(int s=0;s<=t;s++) acc += K[d*L+s]*bf2f(u[(t-s)*D+d]);
    float y=rbf(acc);
    a[t*D+d]=rbf(y+rbf(bf2f(u[t*D+d])*bf2f(Dsk[d])));
  }
}
__global__ void k_gemm1(const u16* x,const u16* wg,const u16* wu,u16* h){
  int s=blockIdx.y; int f=blockIdx.x*256+threadIdx.x;
  float g=0,u=0; for(int k=0;k<D;k++){ float xv=bf2f(x[s*D+k]); g+=xv*bf2f(wg[k*F+f]); u+=xv*bf2f(wu[k*F+f]); }
  h[(size_t)s*F+f]=f2bf(rbf(g)*rbf(u));
}
__global__ void k_gemm2(const u16* h,const u16* wd,const float* g,const float* a,u16* out){
  int s=blockIdx.x;
  for(int d=threadIdx.x; d<D; d+=blockDim.x){
    float acc=0; for(int k=0;k<F;k++) acc+=bf2f(h[(size_t)s*F+k])*bf2f(wd[k*D+d]);
    float b=rbf(acc); float gg=g[s];
    out[s*D+d]=f2bf(rbf(gg*a[s*D+d])+rbf(rbf(1.f-gg)*b));
  }
}
extern "C" void kernel_launch(void* const* d_in, const int* in_sizes, int n_in, void* d_out, int out_size, void* d_ws, size_t ws_size, hipStream_t stream) {
  const u16* u=(const u16*)d_in[0];
  char* ws=(char*)d_ws;
  float* g=(float*)ws; float* K=(float*)(ws+(1<<20)); float* a=(float*)(ws+(8<<20)); u16* h=(u16*)(ws+(16<<20));
  k_gate<<<L,64,0,stream>>>(u,(const u16*)d_in[13],g);
  k_kgen<<<D,256,0,stream>>>((const u16*)d_in[1],(const u16*)d_in[2],(const u16*)d_in[3],(const u16*)d_in[4],(const u16*)d_in[5],(const u16*)d_in[6],(const u16*)d_in[7],K);
  k_conv<<<L,192,0,stream>>>(u,K,(const u16*)d_in[8],a);
  k_gemm1<<<dim3(F/256,L),256,0,stream>>>(u,(const u16*)d_in[10],(const u16*)d_in[11],h);
  k_gemm2<<<L,192,0,stream>>>(h,(const u16*)d_in[12],g,a,(u16*)d_out);
}
```

```cpp
#include <hip/hip_runtime.h>
#include <hip/hip_bf16.h>
#include <stdint.h>
#include <type_traits>

typedef unsigned short u16;
typedef short s16x4 __attribute__((ext_vector_type(4)));
typedef short s16x8 __attribute__((ext_vector_type(8)));
typedef __bf16 bf16x8 __attribute__((ext_vector_type(8)));
typedef __bf16 bf16x2 __attribute__((ext_vector_type(2)));
typedef float f32x2 __attribute__((ext_vector_type(2)));
typedef float f32x4 __attribute__((ext_vector_type(4)));
typedef float f32x16 __attribute__((ext_vector_type(16)));
typedef unsigned u32x4 __attribute__((ext_vector_type(4)));
#define LAS __attribute__((address_space(3)))
#define GAS __attribute__((address_space(1)))

static __device__ __forceinline__ float bf2f(u16 v){ return __uint_as_float(((unsigned)v)<<16); }
static __device__ __forceinline__ u16 f2bf(float f){ __hip_bfloat16 b = __float2bfloat16(f); return *reinterpret_cast<u16*>(&b); }
static __device__ __forceinline__ float rbf(float f){ return bf2f(f2bf(f)); }
static __device__ __forceinline__ unsigned pk_bf16(float a, float b){ f32x2 v={a,b}; bf16x2 r=__builtin_convertvector(v,bf16x2); return __builtin_bit_cast(unsigned,r); }

constexpr int L=2048, D=576, NS=128, F=4608;

__device__ __forceinline__ void glds16(const void* sbase, unsigned voff, unsigned lds_addr){
  unsigned keep;
  asm volatile("s_mov_b32 %0, m0\n\ts_mov_b32 m0, %3\n\ts_nop 0\n\tglobal_load_lds_dwordx4 %1, %2\n\ts_mov_b32 m0, %0"
     : "=&s"(keep) : "v"(voff), "s"(sbase), "s"(lds_addr) : "memory");
}
#define WAIT_VM(n) __builtin_amdgcn_s_waitcnt(((n)&0xF)|(((n)>>4)<<14)|(0x7<<4)|(0xF<<8))
#define BAR() asm volatile("s_barrier" ::: "memory")

#define SB0() __builtin_amdgcn_sched_barrier(0)
template<int N> __device__ __forceinline__ void wait_vm(){ asm volatile("s_waitcnt vmcnt(%0)" :: "n"(N) : "memory"); }
typedef s16x4 frag_half;
struct AFrag { s16x4 lo, hi; };
__device__ __forceinline__ bf16x8 mkfrag(const AFrag& f){ return __builtin_bit_cast(bf16x8, __builtin_shufflevector(f.lo,f.hi,0,1,2,3,4,5,6,7)); }

constexpr int G1_XB = 256*64;
constexpr int G1_WB = 32*288;
constexpr int G1_WG_OFF = G1_XB;
constexpr int G1_WU_OFF = G1_XB + G1_WB + 128;
constexpr int G1_SUB = 35072;
constexpr int G1_LDS = 4*G1_SUB;

__global__ __launch_bounds__(512,2) void k_gemm1(const u16* __restrict__ x, const u16* __restrict__ wg, const u16* __restrict__ wu, u16* __restrict__ hfrag, char* stamp_buf){

  extern __shared__ __attribute__((aligned(256))) char smem_[];
  LAS char* smem = (LAS char*)smem_;
  const int bid = blockIdx.x;
  const int xcd = bid & 7, idx = bid >> 3;
  const int mt = 4*(xcd&1) + (idx&3), ft = 8*(xcd>>1) + (idx>>2);
  const int tid = threadIdx.x, lane = tid & 63;
  const int w = __builtin_amdgcn_readfirstlane(tid >> 6);
  const int m0 = mt*256, f0 = ft*144;
  const unsigned lds0 = __builtin_amdgcn_readfirstlane((unsigned)(size_t)smem);
  unsigned voff[5]; unsigned ldst[5]; const char* gbase[5]; unsigned gstep[5];
  #pragma unroll
  for(int i=0;i<2;i++){
    int ins = w*2+i; int row = ins*16 + (lane>>2); int c = (lane&3) ^ ((row>>2)&3);
    voff[i] = (m0+row)*1152 + c*16; ldst[i] = ins*1024; gbase[i]=(const char*)x; gstep[i]=64;
  }
  #pragma unroll
  for(int i=0;i<3;i++){
    int j = (i<2)? (w + 8*i) : (16 + (w&1));
    int mat = j/9, jj = j%9;
    int c = jj*64 + lane; int row = c/18, col8 = c%18;
    voff[2+i] = row*9216 + (f0 + col8*8)*2;
    gbase[2+i] = (const char*)(mat? wu: wg); gstep[2+i]=32*9216;
    ldst[2+i] = (mat? G1_WU_OFF: G1_WG_OFF) + jj*1024;
  }
  auto issue1 = [&](int i, int sub){
    glds16(gbase[i] + (size_t)sub*gstep[i], voff[i], lds0 + (sub&3)*G1_SUB + ldst[i]);
  };
  const int h = lane>>5, blk=(lane>>4)&1, q=(lane&15)>>2, p=lane&3;
  const int a_base = (blk? G1_WU_OFF: G1_WG_OFF) + (8*h+q)*288 + 8*p;
  int b_addr[2];
  { int r = 32*w + (lane&31);
    #pragma unroll
    for(int s=0;s<2;s++) b_addr[s] = r*64 + (((2*s+h) ^ ((r>>2)&3))*16); }
  f32x16 acc[9];
  #pragma unroll
  for(int i=0;i<9;i++) acc[i] = (f32x16){0,0,0,0,0,0,0,0,0,0,0,0,0,0,0,0};
  AFrag A[6]; bf16x8 B0, B1;
  auto rdA = [&](AFrag& f, LAS char* sb, int fi){
    int s = fi/9, t = fi%9;
    f.lo = __builtin_amdgcn_ds_read_tr16_b64_v4i16((LAS s16x4*)(sb + a_base + s*16*288 + t*32));
    f.hi = __builtin_amdgcn_ds_read_tr16_b64_v4i16((LAS s16x4*)(sb + a_base + s*16*288 + 4*288 + t*32));
  };
  #pragma unroll
  for(int sub=0;sub<3;sub++){
    #pragma unroll
    for(int i=0;i<4;i++) issue1(i,sub);
    if(w<2) issue1(4,sub);
  }
  if(w<2) wait_vm<10>(); else wait_vm<8>();
  BAR();

  B0 = *(LAS bf16x8*)(smem + b_addr[0]);
  #pragma unroll
  for(int i=0;i<6;i++) rdA(A[i], smem, i);
  SB0();
  auto body = [&](int j, bool do_issue){
    LAS char* cur = smem + (j&3)*G1_SUB;
    LAS char* nxt = smem + ((j+1)&3)*G1_SUB;
    #pragma unroll
    for(int i=0;i<18;i++){
      const int t = i%9;
      acc[t] = __builtin_amdgcn_mfma_f32_32x32x16_bf16(mkfrag(A[i%6]), (i<9)? B0 : B1, acc[t],0,0,0);
      if(i+6<18) rdA(A[i%6], cur, i+6); else rdA(A[i%6], nxt, i+6-18);
      if(i==2)  B1 = *(LAS bf16x8*)(cur + b_addr[1]);
      if(i==11) B0 = *(LAS bf16x8*)(nxt + b_addr[0]);
      if(do_issue){
        if(i==0) issue1(0,j+3);
        if(i==3) issue1(1,j+3);
        if(i==6) issue1(2,j+3);
        if(i==9) issue1(3,j+3);
        if(i==12 && w<2) issue1(4,j+3);
      }
      SB0();
    }
  };
  #pragma unroll 1
  for(int j=0;j<15;j++){
    if(w<2) wait_vm<5>(); else wait_vm<4>();
    BAR();

    body(j,true);
  }
  if(w<2) wait_vm<5>(); else wait_vm<4>();
  BAR();
  body(15,false);
  wait_vm<0>(); BAR();
  body(16,false);
  BAR();
  body(17,false);

  const int tt16 = (m0>>4) + 2*w + ((lane>>4)&1);
  #pragma unroll
  for(int t=0;t<9;t++){
    int Ft = ft*9 + t; int fs = Ft>>1, half = Ft&1; int qq = 2*half + h;
    u32x4 v;
    v[0]=pk_bf16(acc[t][0]*acc[t][8],  acc[t][1]*acc[t][9]);
    v[1]=pk_bf16(acc[t][2]*acc[t][10], acc[t][3]*acc[t][11]);
    v[2]=pk_bf16(acc[t][4]*acc[t][12], acc[t][5]*acc[t][13]);
    v[3]=pk_bf16(acc[t][6]*acc[t][14], acc[t][7]*acc[t][15]);
    *(u32x4*)((char*)hfrag + ((size_t)(tt16*144 + fs)*64 + 16*qq + (lane&15))*16) = v;
  }

}

#define G2_NB 6
constexpr int G2_SUB = 64*288;
constexpr int G2_LDS = G2_NB*G2_SUB;
constexpr int G2_RH = 6;
__device__ __forceinline__ void gload16(bf16x8& dst, const void* sbase, unsigned voff){
  asm volatile("global_load_dwordx4 %0, %1, %2" : "=v"(dst) : "v"(voff), "s"(sbase) : "memory");
}
template<int NW> struct G2W {
  static constexpr int PER = NW+4;
  static constexpr int N1 = 4 + (G2_NB-3)*PER, N2 = (G2_RH-2)*PER;
  static constexpr int N = N1<N2? N1: N2;
};
__global__ __launch_bounds__(256,2) void k_gemm2(const u16* __restrict__ hfrag, const u16* __restrict__ wd, float* __restrict__ part, char* stamp_buf){

  extern __shared__ __attribute__((aligned(256))) char smem_[];
  LAS char* smem = (LAS char*)smem_;
  const int bid = blockIdx.x;
  const int xcd = bid & 7, idx = bid >> 3;
  const int tm = 8*(xcd&1) + (idx>>2), dn = idx&3, sk = xcd>>1;
  const int tid = threadIdx.x, lane = tid & 63;
  const int w = __builtin_amdgcn_readfirstlane(tid >> 6);
  const unsigned lds0 = __builtin_amdgcn_readfirstlane((unsigned)(size_t)smem);
  unsigned wsrc[5]; unsigned wdst[5];
  #pragma unroll
  for(int i=0;i<5;i++){
    int j = (i<4)? (w + 4*i) : (16 + (w&1)); int c = j*64+lane; int row = c/18, col8 = c%18;
    wsrc[i] = (sk*1152 + row)*1152 + (dn*144 + col8*8)*2; wdst[i] = j*1024;
  }
  const char* hbase = (const char*)hfrag + (size_t)((tm*8 + 2*w)*144 + sk*36)*1024;
  const unsigned hoff0 = lane*16, hoff1 = lane*16 + 144*1024;
  auto issueW1 = [&](int i, int sub, int buf){
    glds16((const char*)wd + (size_t)sub*64*1152, wsrc[i], lds0 + buf*G2_SUB + wdst[i]);
  };
  bf16x8 hb[G2_RH][4];
  auto issueH1 = [&](int i, int sub, bf16x8& dst){
    gload16(dst, hbase + (size_t)(sub*2 + (i>>1))*1024, (i&1)? hoff1 : hoff0);
  };
  const int qg = lane>>4, q=(lane&15)>>2, p=lane&3;
  const int a_base = (16*(qg>>1) + 4*(qg&1) + q)*288 + 8*p;
  f32x4 acc[2][9];
  #pragma unroll
  for(int i=0;i<2;i++)
  #pragma unroll
  for(int t=0;t<9;t++) acc[i][t] = (f32x4){0,0,0,0};
  AFrag A[6];
  auto rdA = [&](AFrag& f, LAS char* sb, int fi){
    int s = fi/9, t = fi%9;
    f.lo = __builtin_amdgcn_ds_read_tr16_b64_v4i16((LAS s16x4*)(sb + a_base + s*32*288 + t*32));
    f.hi = __builtin_amdgcn_ds_read_tr16_b64_v4i16((LAS s16x4*)(sb + a_base + s*32*288 + 8*288 + t*32));
  };
  {
    constexpr int V0 = -( (G2_NB>G2_RH? G2_NB: G2_RH) -1);
    #pragma unroll
    for(int v=V0; v<0; v++){
      if(v+G2_NB-1>=0){
        #pragma unroll
        for(int i=0;i<4;i++) issueW1(i, v+G2_NB-1, (v+G2_NB-1)%G2_NB);
        if(w<2) issueW1(4, v+G2_NB-1, (v+G2_NB-1)%G2_NB);
      }
      if(v+G2_RH-1>=0){
        #pragma unroll
        for(int i=0;i<4;i++) issueH1(i, v+G2_RH-1, hb[(v+G2_RH-1)%G2_RH][i]);
      }
    }
  }
  {
    if(w<2) wait_vm<G2W<5>::N>(); else wait_vm<G2W<4>::N>();
  }
  BAR();

  #pragma unroll
  for(int i=0;i<6;i++) rdA(A[i], smem, i);
  SB0();
  int cbuf = 0;
  auto body = [&](auto JJ, int j, bool issW, bool issH){
    constexpr int jj = decltype(JJ)::value;
    int nbuf = cbuf+1==G2_NB? 0: cbuf+1;
    int pbuf = cbuf==0? G2_NB-1: cbuf-1;
    LAS char* cur = smem + cbuf*G2_SUB;
    LAS char* nxt = smem + nbuf*G2_SUB;
    asm volatile("" : "+v"(hb[jj][0]), "+v"(hb[jj][1]), "+v"(hb[jj][2]), "+v"(hb[jj][3]));
    #pragma unroll
    for(int a=0;a<18;a++){
      const int fsl = a/9, t = a%9;
      bf16x8 Af = mkfrag(A[a%6]);
      acc[0][t] = __builtin_amdgcn_mfma_f32_16x16x32_bf16(Af, hb[jj][2*fsl],   acc[0][t],0,0,0);
      acc[1][t] = __builtin_amdgcn_mfma_f32_16x16x32_bf16(Af, hb[jj][2*fsl+1], acc[1][t],0,0,0);
      if(a+6<18) rdA(A[a%6], cur, a+6); else rdA(A[a%6], nxt, a+6-18);
      if(issW){
        if(a==0) issueW1(0,j+G2_NB-1,pbuf);
        if(a==2) issueW1(1,j+G2_NB-1,pbuf);
        if(a==4) issueW1(2,j+G2_NB-1,pbuf);
        if(a==6) issueW1(3,j+G2_NB-1,pbuf);
        if(a==8 && w<2) issueW1(4,j+G2_NB-1,pbuf);
      }
      if(issH){
        constexpr int pj = (jj+G2_RH-1)%G2_RH;
        if(a==10) issueH1(0,j+G2_RH-1,hb[pj][0]);
        if(a==12) issueH1(1,j+G2_RH-1,hb[pj][1]);
        if(a==14) issueH1(2,j+G2_RH-1,hb[pj][2]);
        if(a==16) issueH1(3,j+G2_RH-1,hb[pj][3]);
      }
      SB0();
    }
    cbuf = nbuf;
  };
  #define IC(n) std::integral_constant<int,n>{}
  #define G2_TOP() do{ if(w<2) wait_vm<G2W<5>::N>(); else wait_vm<G2W<4>::N>(); BAR(); }while(0)
  #pragma unroll 1
  for(int jb=0;jb<12;jb+=6){
    G2_TOP();  body(IC(0), jb+0, true, true);
    G2_TOP(); body(IC(1), jb+1, true, true);
    G2_TOP();  body(IC(2), jb+2, true, true);
    G2_TOP(); body(IC(3), jb+3, true, true);
    G2_TOP();  body(IC(4), jb+4, true, true);
    G2_TOP(); body(IC(5), jb+5, true, true);
  }
  G2_TOP();  body(IC(0), 12, (12+G2_NB-1<=17), true);
  wait_vm<G2W<4>::PER*2>(); BAR(); body(IC(1), 13, (13+G2_NB-1<=17), false);
  wait_vm<G2W<4>::PER>(); BAR();  body(IC(2), 14, (14+G2_NB-1<=17), false);
  wait_vm<0>(); BAR(); body(IC(3), 15, (15+G2_NB-1<=17), false);
  wait_vm<0>(); BAR();  body(IC(4), 16, false, false);
  wait_vm<0>(); BAR(); body(IC(5), 17, false, false);

  float* pp = part + ((((size_t)(sk*16+tm)*4+dn)*4+w)*18)*256 + lane*4;
  #pragma unroll
  for(int i=0;i<2;i++)
  #pragma unroll
  for(int t=0;t<9;t++) *(f32x4*)(pp + (i*9+t)*256) = acc[i][t];

}

typedef _Float16 f16x8 __attribute__((ext_vector_type(8)));
typedef _Float16 f16x2 __attribute__((ext_vector_type(2)));
typedef unsigned u32x2 __attribute__((ext_vector_type(2)));
__device__ __forceinline__ unsigned pk_f16(float a, float b){ f32x2 v={a,b}; f16x2 r=__builtin_convertvector(v,f16x2); return __builtin_bit_cast(unsigned,r); }
struct cf { float r, i; };
__device__ __forceinline__ cf cmul(cf a, cf b){ cf o; o.r = a.r*b.r - a.i*b.i; o.i = a.r*b.i + a.i*b.r; return o; }
__device__ __forceinline__ cf csqr(cf a){ cf o; o.r = a.r*a.r - a.i*a.i; o.i = 2.f*a.r*a.i; return o; }
__device__ __forceinline__ cf cexpl(float zr2, float zi2, float l){
  float e = __builtin_amdgcn_exp2f(zr2*l);
  float th = __builtin_amdgcn_fractf(zi2*l);
  cf o; o.r = e*__builtin_amdgcn_cosf(th); o.i = e*__builtin_amdgcn_sinf(th); return o;
}
__device__ __forceinline__ float wave_max(float v){
  #pragma unroll
  for(int o=32;o>0;o>>=1) v = fmaxf(v, __shfl_xor(v,o));
  return v;
}
constexpr int S_IDX   = 0;
constexpr int S_UMAX  = 4224;
constexpr int S_WAVE0 = 4352;
constexpr int SW_U    = 0;
constexpr int SW_T    = 4608;
constexpr int SW_KD   = 4608 + 17408;
constexpr int SW_SIZE = 4608 + 17408 + 1344;
constexpr int SSM_LDS = S_WAVE0 + 4*SW_SIZE;
constexpr int SSM_WGS = 144, GATE_WGS = 32;

__device__ __forceinline__ void gate_body(int gb, const u16* __restrict__ x, const u16* __restrict__ gw, float* __restrict__ g){
  const int lane = threadIdx.x&63, w = threadIdx.x>>6;
  u32x4 g0 = *(const u32x4*)(gw + lane*8);
  u32x4 g1 = (lane<8)? *(const u32x4*)(gw + 512 + lane*8) : (u32x4){0,0,0,0};
  #pragma unroll 4
  for(int i=0;i<16;i++){
    int t = gb*64 + w*16 + i;
    u32x4 x0 = *(const u32x4*)(x + t*D + lane*8);
    u32x4 x1 = (lane<8)? *(const u32x4*)(x + t*D + 512 + lane*8) : (u32x4){0,0,0,0};
    float s=0.f;
    #pragma unroll
    for(int k=0;k<4;k++){
      s += rbf(__uint_as_float(x0[k]<<16)*__uint_as_float(g0[k]<<16));
      s += rbf(__uint_as_float(x0[k]&0xffff0000u)*__uint_as_float(g0[k]&0xffff0000u));
      s += rbf(__uint_as_float(x1[k]<<16)*__uint_as_float(g1[k]<<16));
      s += rbf(__uint_as_float(x1[k]&0xffff0000u)*__uint_as_float(g1[k]&0xffff0000u));
    }
    #pragma unroll
    for(int o=32;o>0;o>>=1) s += __shfl_xor(s,o);
    if(lane==0){ float z=rbf(s); g[t]=rbf(1.f/(1.f+__expf(-z))); }
  }
}

__device__ __forceinline__ void ssm_body(int sb, LAS char* smem, const u16* __restrict__ u,
     const u16* __restrict__ Are,const u16* __restrict__ Aim,const u16* __restrict__ Bre,const u16* __restrict__ Bim,
     const u16* __restrict__ Cre,const u16* __restrict__ Cim,const u16* __restrict__ ldt,const u16* __restrict__ Dsk, u16* __restrict__ aT){
  const int tid = threadIdx.x, lane = tid&63;
  const int w = __builtin_amdgcn_readfirstlane(tid>>6);
  const int d0 = sb*4, d = d0 + w;
  LAS char* wv = smem + S_WAVE0 + w*SW_SIZE;
  u32x2 uraw[8];
  #pragma unroll
  for(int i=0;i<8;i++) uraw[i] = *(const u32x2*)(u + (size_t)(tid + 256*i)*D + d0);
  unsigned pr[2][6];
  #pragma unroll
  for(int hh=0;hh<2;hh++){
    int o = d*NS + lane + 64*hh;
    pr[hh][0]=Are[o]; pr[hh][1]=Aim[o]; pr[hh][2]=Bre[o]; pr[hh][3]=Bim[o]; pr[hh][4]=Cre[o]; pr[hh][5]=Cim[o];
  }
  const float dt = rbf(expf(bf2f(ldt[d])));
  const float dsk = bf2f(Dsk[d]);
  for(int e=tid; e<2088; e+=256){
    int s = e-19; int idx;
    if(s<0 || s>2047) idx = 641;
    else if(s<256) idx = s;
    else idx = (int)f2bf((float)s) - 17024;
    *(LAS u16*)(smem + S_IDX + e*2) = (u16)(idx*2);
  }
  {
    unsigned m01=0, m23=0;
    typedef unsigned short u16x2 __attribute__((ext_vector_type(2)));
    #pragma unroll
    for(int i=0;i<8;i++){
      u16x2 a = __builtin_bit_cast(u16x2, uraw[i][0] & 0x7fff7fffu), b = __builtin_bit_cast(u16x2, uraw[i][1] & 0x7fff7fffu);
      m01 = __builtin_bit_cast(unsigned, __builtin_elementwise_max(__builtin_bit_cast(u16x2,m01), a));
      m23 = __builtin_bit_cast(unsigned, __builtin_elementwise_max(__builtin_bit_cast(u16x2,m23), b));
    }
    #pragma unroll
    for(int o=32;o>0;o>>=1){
      unsigned a=__shfl_xor(m01,o), b=__shfl_xor(m23,o);
      m01 = __builtin_bit_cast(unsigned, __builtin_elementwise_max(__builtin_bit_cast(u16x2,m01), __builtin_bit_cast(u16x2,a)));
      m23 = __builtin_bit_cast(unsigned, __builtin_elementwise_max(__builtin_bit_cast(u16x2,m23), __builtin_bit_cast(u16x2,b)));
    }
    if(lane==0){ *(LAS u32x2*)(smem + S_UMAX + w*8) = (u32x2){m01,m23}; }
  }
  if(lane<60) *(LAS u32x2*)(wv + SW_U + lane*8) = (u32x2){0,0};
  __syncthreads();
  int eu[4];
  {
    unsigned m[4]={0,0,0,0};
    #pragma unroll
    for(int ww=0;ww<4;ww++){ u32x2 t = *(LAS u32x2*)(smem + S_UMAX + ww*8);
      m[0]=max(m[0],t[0]&0xffffu); m[1]=max(m[1],t[0]>>16); m[2]=max(m[2],t[1]&0xffffu); m[3]=max(m[3],t[1]>>16); }
    #pragma unroll
    for(int c=0;c<4;c++) eu[c] = (int)(m[c]>>7);
  }
  #pragma unroll
  for(int c=0;c<4;c++){
    int se = 268 - eu[c]; se = se>254?254:(se<1?1:se);
    float sc = __uint_as_float((unsigned)se<<23);
    LAS char* ud = smem + S_WAVE0 + c*SW_SIZE + SW_U + 480;
    #pragma unroll
    for(int i=0;i<8;i++){
      unsigned raw = uraw[i][c>>1]; float v = __uint_as_float((c&1)? (raw&0xffff0000u) : (raw<<16));
      *(LAS _Float16*)(ud + (tid+256*i)*2) = (_Float16)(v*sc);
    }
  }
  int seu; { int e = (w==0)?eu[0]:(w==1)?eu[1]:(w==2)?eu[2]:eu[3]; seu = 268-e; seu = seu>254?254:(seu<1?1:seu); }
  float zr2[2], zi2[2]; cf co[2];
  float cmax=0.f;
  #pragma unroll
  for(int hh=0;hh<2;hh++){
    float are=__uint_as_float(pr[hh][0]<<16), aim=__uint_as_float(pr[hh][1]<<16);
    float br=dt*__uint_as_float(pr[hh][2]<<16), bi=dt*__uint_as_float(pr[hh][3]<<16);
    float cr=__uint_as_float(pr[hh][4]<<16), ci=__uint_as_float(pr[hh][5]<<16);
    zr2[hh] = (dt*are)*1.44269504088896341f; zi2[hh] = (dt*aim)*0.15915494309189535f;
    co[hh].r = cr*br-ci*bi; co[hh].i = cr*bi+ci*br;
    cmax = fmaxf(cmax, fmaxf(fabsf(co[hh].r), fabsf(co[hh].i)));
  }
  cmax = wave_max(cmax);
  int ec = (int)(__float_as_uint(cmax)>>23); int sec = 127+8-(ec-127); sec = sec>254?254:(sec<1?1:sec);
  const float csc = __uint_as_float((unsigned)sec<<23);
  LAS char* T = wv + SW_T;
  for(int i=lane;i<11*68;i+=64) *(LAS unsigned*)(T + 21*272 + i*4) = 0u;
  f32x4 kacc[2][2];
  #pragma unroll
  for(int a=0;a<2;a++) for(int b=0;b<2;b++) kacc[a][b]=(f32x4){0,0,0,0};
  const int fr = lane&15, fq = lane>>4;
  #pragma unroll
  for(int hh=0;hh<2;hh++){
    const float zr=zr2[hh], zi=zi2[hh];
    cf c; c.r=co[hh].r*csc; c.i=co[hh].i*csc;
    cf E1=cexpl(zr,zi,1.f), E2=csqr(E1), E4=csqr(E2);
    cf E8=cexpl(zr,zi,8.f), E16=csqr(E8), E32=csqr(E16);
    cf E64=cexpl(zr,zi,64.f), E128=csqr(E64), E256=csqr(E128);
    cf E512=cexpl(zr,zi,512.f), E1024=csqr(E512);
    LAS char* tw = T + lane*4;
    #define PUTW(row,val) *(LAS unsigned*)(tw + (row)*272) = pk_f16((val).r, -(val).i)
    #define PUTV(row,val) *(LAS unsigned*)(tw + (32+(row))*272) = pk_f16((val).r, (val).i)
    cf W1=cmul(c,E1), W2=cmul(c,E2), W3=cmul(W1,E2);
    PUTW(0,c); PUTW(1,W1); PUTW(2,W2); PUTW(3,W3);
    PUTW(4,cmul(c,E4)); PUTW(5,cmul(W1,E4)); PUTW(6,cmul(W2,E4)); PUTW(7,cmul(W3,E4));
    cf W256=cmul(c,E256), W258=cmul(W256,E2);
    PUTW(8,W256); PUTW(9,W258); PUTW(10,cmul(W256,E4)); PUTW(11,cmul(W258,E4));
    cf W512=cmul(c,E512), W768=cmul(W512,E256);
    PUTW(12,W512); PUTW(13,cmul(W512,E4)); PUTW(14,W768); PUTW(15,cmul(W768,E4));
    cf W1024=cmul(c,E1024), W1536=cmul(W1024,E512);
    PUTW(16,W1024); PUTW(17,cmul(W1024,E256)); PUTW(18,W1536); PUTW(19,cmul(W1536,E256)); PUTW(20,cmul(W1024,E1024));
    cf V[16]; V[0].r=1.f; V[0].i=0.f; V[1]=E8; V[2]=E16; V[3]=cmul(E8,E16);
    V[4]=E32; V[5]=cmul(E32,V[1]); V[6]=cmul(E32,V[2]); V[7]=cmul(E32,V[3]);
    V[8]=E64;
    #pragma unroll
    for(int j=1;j<8;j++) V[8+j]=cmul(E64,V[j]);
    #pragma unroll
    for(int j=0;j<16;j++){ PUTV(j,V[j]); cf t = (j==0)? E128 : cmul(E128,V[j]); PUTV(16+j,t); }
    #pragma unroll
    for(int s=0;s<4;s++){
      f16x8 a0 = *(LAS f16x8*)(T + fr*272 + (4*s+fq)*16);
      f16x8 a1 = *(LAS f16x8*)(T + (16+fr)*272 + (4*s+fq)*16);
      f16x8 b0 = *(LAS f16x8*)(T + (32+fr)*272 + (4*s+fq)*16);
      f16x8 b1 = *(LAS f16x8*)(T + (48+fr)*272 + (4*s+fq)*16);
      kacc[0][0] = __builtin_amdgcn_mfma_f32_16x16x32_f16(a0,b0,kacc[0][0],0,0,0);
      kacc[0][1] = __builtin_amdgcn_mfma_f32_16x16x32_f16(a0,b1,kacc[0][1],0,0,0);
      kacc[1][0] = __builtin_amdgcn_mfma_f32_16x16x32_f16(a1,b0,kacc[1][0],0,0,0);
      kacc[1][1] = __builtin_amdgcn_mfma_f32_16x16x32_f16(a1,b1,kacc[1][1],0,0,0);
    }
  }
  float kmax=0.f;
  #pragma unroll
  for(int a=0;a<2;a++) for(int b=0;b<2;b++) for(int r=0;r<4;r++) kmax = fmaxf(kmax, fabsf(kacc[a][b][r]));
  kmax = wave_max(kmax);
  int ek = (int)(__float_as_uint(kmax)>>23); int sek = 127+13-(ek-127); sek = sek>254?254:(sek<1?1:sek);
  const float ksc = __uint_as_float((unsigned)sek<<23);
  LAS char* KD = wv + SW_KD;
  {
    int base0 = (fq==0)?0:(fq==1)?4:(fq==2)?256:384;
    int js0 = (fq<2)?8:(fq==2)?4:2;
    #pragma unroll
    for(int ml=0;ml<2;ml++){
      int j = fr + 16*ml;
      #pragma unroll
      for(int r=0;r<4;r++){
        int ro = (fq==3)? ((r&1) + 64*(r>>1)) : r;
        *(LAS _Float16*)(KD + (base0 + ro + j*js0)*2) = (_Float16)(kacc[0][ml][r]*ksc);
        if(fq==0) *(LAS _Float16*)(KD + (512 + 32*r + j)*2) = (_Float16)(kacc[1][ml][r]*ksc);
      }
    }
    if(lane==16) *(LAS _Float16*)(KD + 640*2) = (_Float16)(kacc[1][0][0]*ksc);
    if(lane==17) *(LAS u16*)(KD + 641*2) = 0;
  }
  asm volatile("" ::: "memory");
  #pragma unroll 3
  for(int it=0; it<9; it++){
    int X = it*64 + lane;
    if(X<520){
      u32x2 i0 = *(LAS u32x2*)(smem + S_IDX + (2076-4*X)*2);
      u32x2 i1 = *(LAS u32x2*)(smem + S_IDX + (2080-4*X)*2);
      unsigned v[7];
      v[0]=*(LAS u16*)(KD + (i0[0]&0xffffu)); v[1]=*(LAS u16*)(KD + (i0[0]>>16));
      v[2]=*(LAS u16*)(KD + (i0[1]&0xffffu)); v[3]=*(LAS u16*)(KD + (i0[1]>>16));
      v[4]=*(LAS u16*)(KD + (i1[0]&0xffffu)); v[5]=*(LAS u16*)(KD + (i1[0]>>16));
      v[6]=*(LAS u16*)(KD + (i1[1]&0xffffu));
      unsigned P[6];
      #pragma unroll
      for(int a=0;a<6;a++) P[a] = v[a+1] | (v[a]<<16);
      #pragma unroll
      for(int m=0;m<4;m++) *(LAS u32x2*)(T + m*4160 + X*8) = (u32x2){P[m+2], P[m]};
    }
  }
  __syncthreads();
  f32x4 yacc[8];
  #pragma unroll
  for(int c=0;c<8;c++) yacc[c]=(f32x4){0,0,0,0};
  LAS char* abase = T + (fr&3)*4160 + (511 - (fr>>2) + 2*fq)*8;
  LAS char* bbase = wv + SW_U + 2*(16*fr + 8*fq + 224);
  f16x8 Af[2][8], Bf[2][8];
  auto ldfr = [&](int r, f16x8* A, f16x8* B){
    #pragma unroll
    for(int g=0;g<8;g++){
      LAS char* pa = abase - (8*g + r)*64;
      u32x2 lo = *(LAS u32x2*)(pa), hi = *(LAS u32x2*)(pa+8);
      A[g] = __builtin_bit_cast(f16x8, (u32x4){lo[0],lo[1],hi[0],hi[1]});
      B[g] = *(LAS f16x8*)(bbase + (8*g - r)*64);
    }
  };
  ldfr(0, Af[0], Bf[0]);
  #pragma unroll
  for(int r=0;r<8;r++){
    if(r<7) ldfr(r+1, Af[(r+1)&1], Bf[(r+1)&1]);
    #pragma unroll
    for(int g=0;g<8;g++)
    #pragma unroll
    for(int f=0;f<8-g;f++)
      yacc[g+f] = __builtin_amdgcn_mfma_f32_16x16x32_f16(Af[r&1][g], Bf[r&1][f], yacc[g+f],0,0,0);
  }
  const float osc1 = __uint_as_float((unsigned)(254-sec)<<23);
  const float osc2 = __uint_as_float((unsigned)(254-sek)<<23);
  const float usc  = __uint_as_float((unsigned)(254-seu)<<23);
  #pragma unroll
  for(int c=0;c<8;c++){
    int t0 = 256*c + 16*fr + 4*fq;
    typedef _Float16 f16x4 __attribute__((ext_vector_type(4)));
    f16x4 uu = *(LAS f16x4*)(wv + SW_U + 480 + t0*2);
    float o[4];
    #pragma unroll
    for(int r=0;r<4;r++){
      float y = rbf(((yacc[c][r]*osc2)*osc1)*usc);
      float uv = (float)uu[r]*usc;
      o[r] = y + rbf(uv*dsk);
    }
    *(u32x2*)(aT + (size_t)d*L + t0) = (u32x2){pk_bf16(o[0],o[1]), pk_bf16(o[2],o[3])};
  }
}

__global__ __launch_bounds__(256,1) void k_ssm(const u16* __restrict__ u,
     const u16* __restrict__ Are,const u16* __restrict__ Aim,const u16* __restrict__ Bre,const u16* __restrict__ Bim,
     const u16* __restrict__ Cre,const u16* __restrict__ Cim,const u16* __restrict__ ldt,const u16* __restrict__ Dsk,
     const u16* __restrict__ gw, u16* __restrict__ aT, float* __restrict__ g){
  extern __shared__ __attribute__((aligned(256))) char smem_[];
  if(blockIdx.x < SSM_WGS) ssm_body(blockIdx.x, (LAS char*)smem_, u, Are,Aim,Bre,Bim,Cre,Cim,ldt,Dsk,aT);
  else gate_body(blockIdx.x-SSM_WGS, u, gw, g);
}

__global__ void k_final(const float* part,const u16* aT,const float* g,u16* out){
  int gid = blockIdx.x*256+threadIdx.x;
  int lane = gid&63; int r = gid>>6; int t=r%9; r/=9; int i=r&1; r>>=1; int w=r&3; r>>=2; int dn=r&3; int tm=r>>2;
  f32x4 s={0,0,0,0};
  #pragma unroll
  for(int sk=0;sk<4;sk++) s += *(const f32x4*)(part + (((((size_t)(sk*16+tm)*4+dn)*4+w)*18 + i*9+t)*64 + lane)*4);
  int tok = tm*128 + w*32 + i*16 + (lane&15); int d0 = dn*144 + t*16 + (lane>>4)*4;
  float gg=g[tok]; float og=rbf(1.f-gg);
  u16 o[4];
  #pragma unroll
  for(int j=0;j<4;j++){ int d=d0+j; float a=bf2f(aT[d*L+tok]); float b=rbf(s[j]); o[j]=f2bf(rbf(gg*a)+rbf(og*b)); }
  *(uint2*)(out + tok*D + d0) = *(uint2*)o;
}

extern "C" void kernel_launch(void* const* d_in, const int* in_sizes, int n_in, void* d_out, int out_size, void* d_ws, size_t ws_size, hipStream_t stream) {
  const u16* u=(const u16*)d_in[0];
  char* ws=(char*)d_ws;
  float* g=(float*)ws; u16* aT=(u16*)(ws+(8<<20)); u16* h=(u16*)(ws+(16<<20)); float* part=(float*)(ws+(40<<20));
  (void)hipFuncSetAttribute((const void*)k_gemm1, hipFuncAttributeMaxDynamicSharedMemorySize, G1_LDS);
  (void)hipFuncSetAttribute((const void*)k_gemm2, hipFuncAttributeMaxDynamicSharedMemorySize, G2_LDS);
  (void)hipFuncSetAttribute((const void*)k_ssm, hipFuncAttributeMaxDynamicSharedMemorySize, SSM_LDS);
  k_ssm<<<SSM_WGS+GATE_WGS,256,SSM_LDS,stream>>>(u,(const u16*)d_in[1],(const u16*)d_in[2],(const u16*)d_in[3],(const u16*)d_in[4],(const u16*)d_in[5],(const u16*)d_in[6],(const u16*)d_in[7],(const u16*)d_in[8],(const u16*)d_in[13],aT,g);
  k_gemm1<<<256,512,G1_LDS,stream>>>(u,(const u16*)d_in[10],(const u16*)d_in[11],h,ws+(100<<20));
  k_gemm2<<<256,256,G2_LDS,stream>>>(h,(const u16*)d_in[12],part,ws+(101<<20));
  k_final<<<16*4*4*18*64/256,256,0,stream>>>(part,aT,g,(u16*)d_out);
}
```

```cpp
#include <hip/hip_runtime.h>
#include <hip/hip_bf16.h>
#include <stdint.h>
#include <type_traits>

typedef unsigned short u16;
typedef short s16x4 __attribute__((ext_vector_type(4)));
typedef short s16x8 __attribute__((ext_vector_type(8)));
typedef __bf16 bf16x8 __attribute__((ext_vector_type(8)));
typedef __bf16 bf16x2 __attribute__((ext_vector_type(2)));
typedef float f32x2 __attribute__((ext_vector_type(2)));
typedef float f32x4 __attribute__((ext_vector_type(4)));
typedef float f32x16 __attribute__((ext_vector_type(16)));
typedef unsigned u32x4 __attribute__((ext_vector_type(4)));
#define LAS __attribute__((address_space(3)))
#define GAS __attribute__((address_space(1)))

static __device__ __forceinline__ float bf2f(u16 v){ return __uint_as_float(((unsigned)v)<<16); }
static __device__ __forceinline__ u16 f2bf(float f){ __hip_bfloat16 b = __float2bfloat16(f); return *reinterpret_cast<u16*>(&b); }
static __device__ __forceinline__ float rbf(float f){ return bf2f(f2bf(f)); }
static __device__ __forceinline__ unsigned pk_bf16(float a, float b){ f32x2 v={a,b}; bf16x2 r=__builtin_convertvector(v,bf16x2); return __builtin_bit_cast(unsigned,r); }

constexpr int L=2048, D=576, NS=128, F=4608;

__device__ __forceinline__ void glds16(const void* sbase, unsigned voff, unsigned lds_addr){
  unsigned keep;
  asm volatile("s_mov_b32 %0, m0\n\ts_mov_b32 m0, %3\n\ts_nop 0\n\tglobal_load_lds_dwordx4 %1, %2\n\ts_mov_b32 m0, %0"
     : "=&s"(keep) : "v"(voff), "s"(sbase), "s"(lds_addr) : "memory");
}
#define WAIT_VM(n) __builtin_amdgcn_s_waitcnt(((n)&0xF)|(((n)>>4)<<14)|(0x7<<4)|(0xF<<8))
#define BAR() asm volatile("s_barrier" ::: "memory")

#define SB0() __builtin_amdgcn_sched_barrier(0)
template<int N> __device__ __forceinline__ void wait_vm(){ asm volatile("s_waitcnt vmcnt(%0)" :: "n"(N) : "memory"); }
typedef s16x4 frag_half;
struct AFrag { s16x4 lo, hi; };
__device__ __forceinline__ bf16x8 mkfrag(const AFrag& f){ return __builtin_bit_cast(bf16x8, __builtin_shufflevector(f.lo,f.hi,0,1,2,3,4,5,6,7)); }

constexpr int G1_XB = 256*64;
constexpr int G1_WB = 32*288;
constexpr int G1_WG_OFF = G1_XB;
constexpr int G1_WU_OFF = G1_XB + G1_WB + 128;
constexpr int G1_SUB = 35072;
constexpr int G1_LDS = 4*G1_SUB;

#define G1_STAG 9
#define G1_GAP 2
#define NSTAMP 0
constexpr int kh_dummy = 0;
typedef _Float16 f16x8 __attribute__((ext_vector_type(8)));
typedef _Float16 f16x2 __attribute__((ext_vector_type(2)));
typedef unsigned u32x2 __attribute__((ext_vector_type(2)));
__device__ __forceinline__ unsigned pk_f16(float a, float b){ f32x2 v={a,b}; f16x2 r=__builtin_convertvector(v,f16x2); return __builtin_bit_cast(unsigned,r); }
struct cf { float r, i; };
__device__ __forceinline__ cf cmul(cf a, cf b){ cf o; o.r = a.r*b.r - a.i*b.i; o.i = a.r*b.i + a.i*b.r; return o; }
__device__ __forceinline__ cf csqr(cf a){ cf o; o.r = a.r*a.r - a.i*a.i; o.i = 2.f*a.r*a.i; return o; }
__device__ __forceinline__ cf cexpl(float zr2, float zi2, float l){
  float e = __builtin_amdgcn_exp2f(zr2*l);
  float th = __builtin_amdgcn_fractf(zi2*l);
  cf o; o.r = e*__builtin_amdgcn_cosf(th); o.i = e*__builtin_amdgcn_sinf(th); return o;
}
__device__ __forceinline__ float wave_max(float v){
  #pragma unroll
  for(int o=32;o>0;o>>=1) v = fmaxf(v, __shfl_xor(v,o));
  return v;
}
constexpr int S_IDX   = 0;
constexpr int S_UMAX  = 4224;
constexpr int S_WAVE0 = 4352;
constexpr int SW_U    = 0;
constexpr int SW_T    = 4608;
constexpr int SW_KD   = 4608 + 17408;
constexpr int SW_SIZE = 4608 + 17408 + 1344;
constexpr int SSM_LDS = S_WAVE0 + 4*SW_SIZE;
constexpr int SSM_WGS = 144, GATE_WGS = 32;

#define JUNK_I 2
#define WD_J0 8
#define H_SC1 1
typedef GAS unsigned long long gu64;
typedef GAS unsigned gu32;
#define RLX_AGENT __ATOMIC_RELAXED, __HIP_MEMORY_SCOPE_AGENT
constexpr unsigned long long TAG_STEP = 0x9E3779B97F4A7C15ull;
constexpr unsigned kSpinCap = 1u<<18;
constexpr size_t WS_E = 0, WS_F1 = 4096, WS_F2 = 4096 + 256*128, WS_F0 = 4096 + 512*128, WS_F3 = 4096 + 768*128, WS_UT = 5<<20, WS_G = 1<<20, WS_AT = 2<<20, WS_H = 8<<20, WS_P = 32<<20;
__device__ __forceinline__ void wait_tags(gu64* p, bool act, unsigned long long tag){
  for(unsigned spins=0; spins<kSpinCap; ++spins){
    unsigned long long v = act? __hip_atomic_load(p, RLX_AGENT) : tag;
    if(__all(v==tag)) return;
    __builtin_amdgcn_s_sleep(1);
  }
}
constexpr int FUSED_LDS = 163840;
constexpr int CODE_KB = 45;
constexpr int G2_RB = 53248;
#define LBAR() asm volatile("s_waitcnt lgkmcnt(0)\n\ts_barrier" ::: "memory")
constexpr int G2_SUB = 64*288;
constexpr int G2_NB = 6, G2_RH = 6;

__device__ __forceinline__ void pf4(const void* p, unsigned lds_addr){
  unsigned keep;
  asm volatile("s_mov_b32 %0, m0\n\ts_mov_b32 m0, %2\n\ts_nop 0\n\tglobal_load_lds_dword %1, off\n\ts_mov_b32 m0, %0"
     : "=&s"(keep) : "v"(p), "s"(lds_addr) : "memory");
}
__device__ __forceinline__ void gload16(bf16x8& dst, const void* sbase, unsigned voff){
  asm volatile("global_load_dwordx4 %0, %1, %2 sc1" : "=v"(dst) : "v"(voff), "s"(sbase) : "memory");
}
constexpr int g2_wait(int j, int nw){
  int total = 3*nw; int posH = 0; int posW = (j+1>=3 && j+1<=5)? (j+1-2)*nw : 0;
  for(int it=1; it<j; ++it){
    if(it+5<=17){ total += nw; if(it+5==j+1) posW = total; }
    if(it+G2_RH-1<=17){ total += 2; if(it+G2_RH-1==j) posH = total; }
  }
  int need = posH>posW? posH: posW;
  return total-need;
}
__device__ __forceinline__ unsigned wave_umax(unsigned v){
  v = max(v, (unsigned)__builtin_amdgcn_update_dpp(0, (int)v, 0x111, 0xf, 0xf, true));
  v = max(v, (unsigned)__builtin_amdgcn_update_dpp(0, (int)v, 0x112, 0xf, 0xf, true));
  v = max(v, (unsigned)__builtin_amdgcn_update_dpp(0, (int)v, 0x114, 0xf, 0xf, true));
  v = max(v, (unsigned)__builtin_amdgcn_update_dpp(0, (int)v, 0x118, 0xf, 0xf, true));
  v = max(v, (unsigned)__builtin_amdgcn_update_dpp(0, (int)v, 0x142, 0xa, 0xf, true));
  v = max(v, (unsigned)__builtin_amdgcn_update_dpp(0, (int)v, 0x143, 0xc, 0xf, true));
  return (unsigned)__builtin_amdgcn_readlane((int)v, 63);
}
struct IdxTab { unsigned short v[2112]; };
constexpr IdxTab make_idx(){
  IdxTab t{};
  for(int e=0;e<2112;e++){
    int s = e-19; int idx = 641;
    if(s>=0 && s<256) idx = s;
    else if(s>=256 && s<=2047){
      int sh = (s<512)?1:(s<1024)?2:3;
      int q = s>>sh, rem = s & ((1<<sh)-1), half = 1<<(sh-1);
      if(rem>half || (rem==half && (q&1))) q++;
      int r = q<<sh;
      idx = (r<=512)? 256 + (r-256)/2 : (r<=1024)? 384 + (r-512)/4 : 512 + (r-1024)/8;
    }
    t.v[e] = (unsigned short)(idx*2);
  }
  return t;
}
__device__ const IdxTab g_idx = make_idx();

struct GateIn { u32x4 g0, g1, x0[4], x1[4]; };
__device__ __forceinline__ void gate_load(GateIn& in, int n, const u16* __restrict__ x, const u16* __restrict__ gw){
  const int lane = threadIdx.x&63, w = threadIdx.x>>6;
  in.g0 = *(const u32x4*)(gw + lane*8);
  in.g1 = (lane<8)? *(const u32x4*)(gw + 512 + lane*8) : (u32x4){0,0,0,0};
  #pragma unroll
  for(int i=0;i<4;i++){
    int t = n*32 + w*4 + i;
    in.x0[i] = *(const u32x4*)(x + t*D + lane*8);
    in.x1[i] = (lane<8)? *(const u32x4*)(x + t*D + 512 + lane*8) : (u32x4){0,0,0,0};
  }
}
__device__ __forceinline__ void gate_phase(const GateIn& in, int n, float* __restrict__ g){
  const int lane = threadIdx.x&63, w = threadIdx.x>>6;
  #pragma unroll
  for(int i=0;i<4;i++){
    int t = n*32 + w*4 + i;
    float s=0.f;
    #pragma unroll
    for(int k=0;k<4;k++){
      s += rbf(__uint_as_float(in.x0[i][k]<<16)*__uint_as_float(in.g0[k]<<16));
      s += rbf(__uint_as_float(in.x0[i][k]&0xffff0000u)*__uint_as_float(in.g0[k]&0xffff0000u));
      s += rbf(__uint_as_float(in.x1[i][k]<<16)*__uint_as_float(in.g1[k]<<16));
      s += rbf(__uint_as_float(in.x1[i][k]&0xffff0000u)*__uint_as_float(in.g1[k]&0xffff0000u));
    }
    #pragma unroll
    for(int o=32;o>0;o>>=1) s += __shfl_xor(s,o);
    if(lane==0){ float z=rbf(s); float gv=rbf(1.f/(1.f+__expf(-z))); __hip_atomic_store((gu32*)(g+t), __float_as_uint(gv), RLX_AGENT); }
  }
}
#define PUTW(row,val) *(LAS unsigned*)(tw + (row)*272) = pk_f16((val).r, -(val).i)
#define PUTV(row,val) *(LAS unsigned*)(tw + (32+(row))*272) = pk_f16((val).r, (val).i)

struct SsmIn { unsigned pr[6]; u32x2 ix0, ix1; };
struct SsmSt { int sec, sek, seu; float dsk; };
constexpr int S_KX = S_UMAX + 64;
__device__ __forceinline__ void ssm_load(SsmIn& in, int d, bool all,
     const u16* __restrict__ Are,const u16* __restrict__ Aim,const u16* __restrict__ Bre,const u16* __restrict__ Bim,
     const u16* __restrict__ Cre,const u16* __restrict__ Cim){
  const int o = d*NS + (threadIdx.x&63)*2;
  in.pr[0]=*(const unsigned*)(Are+o); in.pr[1]=*(const unsigned*)(Aim+o);
  in.pr[2]=0; in.pr[3]=0; in.pr[4]=0; in.pr[5]=0;
  if(all){ in.pr[2]=*(const unsigned*)(Bre+o); in.pr[3]=*(const unsigned*)(Bim+o); in.pr[4]=*(const unsigned*)(Cre+o); in.pr[5]=*(const unsigned*)(Cim+o); }
}
__device__ __forceinline__ void ssm_load_idx(SsmIn& in){
  const int tid = threadIdx.x;
  in.ix0 = *(const u32x2*)((const char*)g_idx.v + tid*8); in.ix1 = *(const u32x2*)((const char*)g_idx.v + 4096 + (tid&15)*8);
}
template<class PUB, class PRE, class STF>
__device__ __forceinline__ void ssm_part1(LAS char* smem, const SsmIn& in, unsigned ldt_lo, unsigned dsk_lo, SsmSt& st, const u16* uT4, PUB pub, PRE pre, STF stf){
  const int tid = threadIdx.x, lane = tid&63;
  const int w8 = __builtin_amdgcn_readfirstlane(tid>>6);
  const int w = w8&3, hb = w8>>2;
  LAS char* wv = smem + S_WAVE0 + w*SW_SIZE;
  LAS char* T = wv + SW_T;
  st.dsk = bf2f((u16)dsk_lo);
  if(hb==0){ if(lane<60) *(LAS u32x2*)(wv + SW_U + lane*8) = (u32x2){0,0}; }
  else { for(int i=lane;i<11*68;i+=64) *(LAS unsigned*)(T + 21*272 + i*4) = 0u; }
  const float dt = rbf(expf(bf2f((u16)ldt_lo)));
  float zr2[2], zi2[2]; cf co[2];
  #pragma unroll
  for(int hh=0;hh<2;hh++){
    float are=__uint_as_float(hh? (in.pr[0]&0xffff0000u) : (in.pr[0]<<16)), aim=__uint_as_float(hh? (in.pr[1]&0xffff0000u) : (in.pr[1]<<16));
    zr2[hh] = (dt*are)*1.44269504088896341f; zi2[hh] = (dt*aim)*0.15915494309189535f;
  }
  int sec = 127;
  if(hb==0){
    float cmax=0.f;
    #pragma unroll
    for(int hh=0;hh<2;hh++){
      float br=dt*__uint_as_float(hh? (in.pr[2]&0xffff0000u) : (in.pr[2]<<16)), bi=dt*__uint_as_float(hh? (in.pr[3]&0xffff0000u) : (in.pr[3]<<16));
      float cr=__uint_as_float(hh? (in.pr[4]&0xffff0000u) : (in.pr[4]<<16)), ci=__uint_as_float(hh? (in.pr[5]&0xffff0000u) : (in.pr[5]<<16));
      co[hh].r = cr*br-ci*bi; co[hh].i = cr*bi+ci*br;
      cmax = fmaxf(cmax, fmaxf(fabsf(co[hh].r), fabsf(co[hh].i)));
    }
    cmax = __uint_as_float(wave_umax(__float_as_uint(cmax)));
    int ec = (int)(__float_as_uint(cmax)>>23); sec = 127+8-(ec-127); sec = sec>254?254:(sec<1?1:sec);
  }
  const float csc = __uint_as_float((unsigned)sec<<23);
  LAS char* tw = T + lane*4;
  auto tables = [&](int hh){
    const float zr=zr2[hh], zi=zi2[hh];
    if(hb==0){
      cf c; c.r=co[hh].r*csc; c.i=co[hh].i*csc;
      cf E1=cexpl(zr,zi,1.f), E2=csqr(E1), E4=csqr(E2);
      cf E256=cexpl(zr,zi,256.f), E512=csqr(E256), E1024=csqr(E512);
      cf W1=cmul(c,E1), W2=cmul(c,E2), W3=cmul(W1,E2);
      PUTW(0,c); PUTW(1,W1); PUTW(2,W2); PUTW(3,W3);
      PUTW(4,cmul(c,E4)); PUTW(5,cmul(W1,E4)); PUTW(6,cmul(W2,E4)); PUTW(7,cmul(W3,E4));
      cf W256=cmul(c,E256), W258=cmul(W256,E2);
      PUTW(8,W256); PUTW(9,W258); PUTW(10,cmul(W256,E4)); PUTW(11,cmul(W258,E4));
      cf W512=cmul(c,E512), W768=cmul(W512,E256);
      PUTW(12,W512); PUTW(13,cmul(W512,E4)); PUTW(14,W768); PUTW(15,cmul(W768,E4));
      cf W1024=cmul(c,E1024), W1536=cmul(W1024,E512);
      PUTW(16,W1024); PUTW(17,cmul(W1024,E256)); PUTW(18,W1536); PUTW(19,cmul(W1536,E256)); PUTW(20,cmul(W1024,E1024));
    } else {
      cf E8=cexpl(zr,zi,8.f), E16=csqr(E8), E32=csqr(E16);
      cf E64=cexpl(zr,zi,64.f), E128=csqr(E64);
      cf V[16]; V[0].r=1.f; V[0].i=0.f; V[1]=E8; V[2]=E16; V[3]=cmul(E8,E16);
      V[4]=E32; V[5]=cmul(E32,V[1]); V[6]=cmul(E32,V[2]); V[7]=cmul(E32,V[3]);
      V[8]=E64;
      #pragma unroll
      for(int j=1;j<8;j++) V[8+j]=cmul(E64,V[j]);
      #pragma unroll
      for(int j=0;j<16;j++){ PUTV(j,V[j]); cf t = (j==0)? E128 : cmul(E128,V[j]); PUTV(16+j,t); }
    }
  };
  const int fr = lane&15, fq = lane>>4;
  f32x4 kacc[2];
  kacc[0]=(f32x4){0,0,0,0}; kacc[1]=(f32x4){0,0,0,0};
  auto kmfma = [&](){
    #pragma unroll
    for(int s=0;s<4;s++){
      f16x8 a  = *(LAS f16x8*)(T + (16*hb+fr)*272 + (4*s+fq)*16);
      f16x8 b0 = *(LAS f16x8*)(T + (32+fr)*272 + (4*s+fq)*16);
      f16x8 b1 = *(LAS f16x8*)(T + (48+fr)*272 + (4*s+fq)*16);
      kacc[0] = __builtin_amdgcn_mfma_f32_16x16x32_f16(a,b0,kacc[0],0,0,0);
      kacc[1] = __builtin_amdgcn_mfma_f32_16x16x32_f16(a,b1,kacc[1],0,0,0);
    }
  };
  tables(0);
  pub();
  *(LAS u32x2*)(smem + S_IDX + tid*8) = in.ix0;
  if(tid<16) *(LAS u32x2*)(smem + S_IDX + 4096 + tid*8) = in.ix1;
  stf();
  LBAR();
  u32x4 ur[2];
  {
    const __amdgpu_buffer_rsrc_t rsU = __builtin_amdgcn_make_buffer_rsrc((void*)uT4, 0, 4*L*2, 0x00020000);
    ur[0] = __builtin_amdgcn_raw_buffer_load_b128(rsU, (hb*L + (tid&255)*8)*2, 0, 16);
    ur[1] = __builtin_amdgcn_raw_buffer_load_b128(rsU, ((hb+2)*L + (tid&255)*8)*2, 0, 16);
  }
  SB0();
  kmfma();
  LBAR();
  stf();
  tables(1);
  {
    typedef unsigned short u16x2 __attribute__((ext_vector_type(2)));
    unsigned mm[2];
    #pragma unroll
    for(int c=0;c<2;c++){
      u16x2 m = __builtin_bit_cast(u16x2, ur[c][0] & 0x7fff7fffu);
      #pragma unroll
      for(int k=1;k<4;k++) m = __builtin_elementwise_max(m, __builtin_bit_cast(u16x2, ur[c][k] & 0x7fff7fffu));
      mm[c] = wave_umax(max((unsigned)m[0], (unsigned)m[1]));
    }
    if(lane==0) *(LAS u32x2*)(smem + S_UMAX + w8*8) = (u32x2){mm[0], mm[1]};
  }
  LBAR();
  pre();
  kmfma();
  int eu[4];
  #pragma unroll
  for(int c=0;c<4;c++){
    unsigned m=0;
    #pragma unroll
    for(int ww=0;ww<4;ww++) m = max(m, *(LAS unsigned*)(smem + S_UMAX + (4*(c&1)+ww)*8 + (c>>1)*4));
    eu[c] = (int)(m>>7);
  }
  #pragma unroll
  for(int c=0;c<2;c++){
    int e = hb? eu[2*c+1] : eu[2*c];
    int se = 268 - e; se = se>254?254:(se<1?1:se);
    float sc = __uint_as_float((unsigned)se<<23);
    u32x4 o;
    #pragma unroll
    for(int k=0;k<4;k++) o[k] = pk_f16(__uint_as_float(ur[c][k]<<16)*sc, __uint_as_float(ur[c][k]&0xffff0000u)*sc);
    *(LAS u32x4*)(smem + S_WAVE0 + (hb+2*c)*SW_SIZE + SW_U + 480 + (tid&255)*16) = o;
  }
  { int e = (w==0)?eu[0]:(w==1)?eu[1]:(w==2)?eu[2]:eu[3]; int seu = 268-e; st.seu = seu>254?254:(seu<1?1:seu); }

  float kmax=0.f;
  #pragma unroll
  for(int b=0;b<2;b++) for(int r=0;r<4;r++) kmax = fmaxf(kmax, fabsf(kacc[b][r]));
  kmax = __uint_as_float(wave_umax(__float_as_uint(kmax)));
  if(lane==0) *(LAS u32x2*)(smem + S_KX + w8*8) = (u32x2){__float_as_uint(kmax), (unsigned)sec};
  LBAR();
  stf();
  {
    u32x2 ka = *(LAS u32x2*)(smem + S_KX + w*8), kb = *(LAS u32x2*)(smem + S_KX + (w+4)*8);
    unsigned km = max(ka[0], kb[0]);
    st.sec = (int)ka[1];
    int ek = (int)(km>>23); int sek = 127+13-(ek-127); st.sek = sek>254?254:(sek<1?1:sek);
  }
  const float ksc = __uint_as_float((unsigned)st.sek<<23);
  LAS char* KD = wv + SW_KD;
  if(hb==0){
    int base0 = (fq==0)?0:(fq==1)?4:(fq==2)?256:384;
    int js0 = (fq<2)?8:(fq==2)?4:2;
    #pragma unroll
    for(int ml=0;ml<2;ml++){
      int j = fr + 16*ml;
      #pragma unroll
      for(int r=0;r<4;r++){
        int ro = (fq==3)? ((r&1) + 64*(r>>1)) : r;
        *(LAS _Float16*)(KD + (base0 + ro + j*js0)*2) = (_Float16)(kacc[ml][r]*ksc);
      }
    }
  } else {
    if(fq==0){
      #pragma unroll
      for(int ml=0;ml<2;ml++)
      #pragma unroll
      for(int r=0;r<4;r++) *(LAS _Float16*)(KD + (512 + 32*r + fr + 16*ml)*2) = (_Float16)(kacc[ml][r]*ksc);
    }
    if(lane==16) *(LAS _Float16*)(KD + 640*2) = (_Float16)(kacc[0][0]*ksc);
    if(lane==17) *(LAS u16*)(KD + 641*2) = 0;
  }
  LBAR();
  stf();
  #pragma unroll
  for(int it=0; it<5; it++){
    int X = it*128 + hb*64 + lane; X = X>519? 519: X;
    {
      u32x2 i0 = *(LAS u32x2*)(smem + S_IDX + (2076-4*X)*2);
      u32x2 i1 = *(LAS u32x2*)(smem + S_IDX + (2080-4*X)*2);
      unsigned v[7];
      v[0]=*(LAS u16*)(KD + (i0[0]&0xffffu)); v[1]=*(LAS u16*)(KD + (i0[0]>>16));
      v[2]=*(LAS u16*)(KD + (i0[1]&0xffffu)); v[3]=*(LAS u16*)(KD + (i0[1]>>16));
      v[4]=*(LAS u16*)(KD + (i1[0]&0xffffu)); v[5]=*(LAS u16*)(KD + (i1[0]>>16));
      v[6]=*(LAS u16*)(KD + (i1[1]&0xffffu));
      unsigned Pk[6];
      #pragma unroll
      for(int a=0;a<6;a++) Pk[a] = v[a+1] | (v[a]<<16);
      #pragma unroll
      for(int m=0;m<4;m++) *(LAS u32x2*)(T + m*4160 + X*8) = (u32x2){Pk[m+2], Pk[m]};
    }
  }
  stf();
}
template<int HB, class HK>
__device__ __forceinline__ void ssm_conv(LAS char* wv, f32x4 (&yacc)[8], HK hook){
  const int lane = threadIdx.x&63, fr = lane&15, fq = lane>>4;
  LAS char* T = wv + SW_T;
  #pragma unroll
  for(int c=0;c<8;c++) yacc[c]=(f32x4){0,0,0,0};
  LAS char* abase = T + (fr&3)*4160 + (511 - (fr>>2) + 2*fq)*8;
  LAS char* abase2 = abase + 8;
  asm volatile("" : "+v"(abase2));
  LAS char* bbase = wv + SW_U + 2*(16*fr + 8*fq + 224);
  f16x8 Af[2][8], Bf[2][8];
  auto ldfr = [&](int r, f16x8* A, f16x8* B){
    #pragma unroll
    for(int g=0;g<8;g++){
      u32x2 lo = *(LAS u32x2*)(abase - (8*g + r)*64), hi = *(LAS u32x2*)(abase2 - (8*g + r)*64);
      A[g] = __builtin_bit_cast(f16x8, (u32x4){lo[0],lo[1],hi[0],hi[1]});
      B[g] = *(LAS f16x8*)(bbase + (8*g - r)*64);
    }
  };
  ldfr(4*HB, Af[0], Bf[0]);
  #pragma unroll
  for(int rr=0;rr<4;rr++){
    if(rr<3) ldfr(4*HB+rr+1, Af[(rr+1)&1], Bf[(rr+1)&1]);
    #pragma unroll
    for(int g=0;g<8;g++){
      #pragma unroll
      for(int f=0;f<8-g;f++)
        yacc[g+f] = __builtin_amdgcn_mfma_f32_16x16x32_f16(Af[rr&1][g], Bf[rr&1][f], yacc[g+f],0,0,0);
      if(rr<2 && g<6){ SB0(); hook(rr*6+g); SB0(); }
    }
  }
}
__device__ __forceinline__ void ssm_out(LAS char* wv, int HB, int d, f32x4 (&yacc)[8], const SsmSt& st, u16* __restrict__ aT){
  const int lane = threadIdx.x&63, fr = lane&15, fq = lane>>4;
  LAS char* T = wv + SW_T;
  #pragma unroll
  for(int k=0;k<4;k++) *(LAS f32x4*)(T + HB*4096 + k*1024 + lane*16) = HB? yacc[k] : yacc[4+k];
  LBAR();
  const float osc1 = __uint_as_float((unsigned)(254-st.sec)<<23);
  const float osc2 = __uint_as_float((unsigned)(254-st.sek)<<23);
  const float usc  = __uint_as_float((unsigned)(254-st.seu)<<23);
  const __amdgpu_buffer_rsrc_t rsA = __builtin_amdgcn_make_buffer_rsrc((void*)aT, 0, D*L*2, 0x00020000);
  #pragma unroll
  for(int k=0;k<4;k++){
    f32x4 y4 = (HB? yacc[4+k] : yacc[k]) + *(LAS f32x4*)(T + (1-HB)*4096 + k*1024 + lane*16);
    int t0 = 256*(4*HB+k) + 16*fr + 4*fq;
    typedef _Float16 f16x4 __attribute__((ext_vector_type(4)));
    f16x4 uu = *(LAS f16x4*)(wv + SW_U + 480 + t0*2);
    float o[4];
    #pragma unroll
    for(int r=0;r<4;r++){
      float y = rbf(((y4[r]*osc2)*osc1)*usc);
      float uv = (float)uu[r]*usc;
      o[r] = y + rbf(uv*st.dsk);
    }
    __builtin_amdgcn_raw_buffer_store_b64((u32x2){pk_bf16(o[0],o[1]), pk_bf16(o[2],o[3])}, rsA, (d*L + t0)*2, 0, 16);
  }
}
struct Params {
  const u16 *u, *Are, *Aim, *Bre, *Bim, *Cre, *Cim, *ldt, *Dsk, *wg, *wu, *wd, *gw;
  u16* out; char* ws;
};
#define IC(n) std::integral_constant<int,n>{}

__global__ __launch_bounds__(512,2) void k_fused(Params P){
  char* const ws = P.ws;

  extern __shared__ __attribute__((aligned(256))) char smem_[];
  LAS char* smem = (LAS char*)smem_;
  const int bid = blockIdx.x;
  const int xcd = bid & 7, idx = bid >> 3;
  const int mt = 4*(xcd&1) + (idx&3), sk = xcd>>1, mem = idx>>2;
  const int ft = 8*sk + mem;
  const bool is_ssm = idx < 18;
  const int sb = xcd*18 + idx;
  const int tid = threadIdx.x, lane = tid & 63;
  const int w = __builtin_amdgcn_readfirstlane(tid >> 6);
  const unsigned lds0 = __builtin_amdgcn_readfirstlane((unsigned)(size_t)smem);
  u16* const hfrag = (u16*)(ws + WS_H);
  unsigned long long tag; unsigned ld_ds;
  SsmIn sin; GateIn gin;
  {
    const u16* x = P.u;
    const int m0 = mt*256, f0 = ft*144;
    unsigned voff[5]; unsigned ldst[5]; const char* gbase[5]; unsigned gstep[5];
    #pragma unroll
    for(int i=0;i<2;i++){
      int ins = w*2+i; int row = ins*16 + (lane>>2); int c = (lane&3) ^ ((row>>2)&3);
      voff[i] = (m0+row)*1152 + c*16; ldst[i] = ins*1024; gbase[i]=(const char*)x; gstep[i]=64;
    }
    #pragma unroll
    for(int i=0;i<3;i++){
      int j = (i<2)? (w + 8*i) : (16 + (w&1));
      int mat = j/9, jj = j%9;
      int c = jj*64 + lane; int row = c/18, col8 = c%18;
      voff[2+i] = row*9216 + (f0 + col8*8)*2;
      gbase[2+i] = (const char*)(mat? P.wu: P.wg); gstep[2+i]=32*9216;
      ldst[2+i] = (mat? G1_WU_OFF: G1_WG_OFF) + jj*1024;
    }
    auto issue1 = [&](int i, int sub){
      glds16(gbase[i] + (size_t)sub*gstep[i], voff[i], lds0 + (sub&3)*G1_SUB + ldst[i]);
    };
    const int h = lane>>5, blk=(lane>>4)&1, q=(lane&15)>>2, p=lane&3;
    const int a_base = (blk? G1_WU_OFF: G1_WG_OFF) + (8*h+q)*288 + 8*p;
    int b_addr[2];
    { int r = 32*w + (lane&31);
      #pragma unroll
      for(int s=0;s<2;s++) b_addr[s] = r*64 + (((2*s+h) ^ ((r>>2)&3))*16); }
    f32x16 acc[9];
    AFrag A[6]; bf16x8 B0, B1;
    auto rdA = [&](AFrag& f, LAS char* sb, int fi){
      int s = fi/9, t = fi%9;
      f.lo = __builtin_amdgcn_ds_read_tr16_b64_v4i16((LAS s16x4*)(sb + a_base + s*16*288 + t*32));
      f.hi = __builtin_amdgcn_ds_read_tr16_b64_v4i16((LAS s16x4*)(sb + a_base + s*16*288 + 4*288 + t*32));
    };
    unsigned long long pc0;
    asm volatile("s_getpc_b64 %0" : "=s"(pc0));
    pc0 &= ~255ull;
    auto warm_code = [&](){
      if(w==7){
        glds16((const char*)pc0 + idx*1024, lane*16, lds0 + 161792);
        if(idx + 32 < CODE_KB) glds16((const char*)pc0 + (idx+32)*1024, lane*16, lds0 + 162816);
      }
    };
    auto junk = [&](int j){
      int ln = lane; asm volatile("" : "+v"(ln));
      const char* pa;
      if(w<6){
        pa = (const char*)x + ((size_t)mt*294912 + ft*9216);
      } else if(w==7){
        int sub2 = (j<0)? 3 + (ln>>5) : j + 5; sub2 = sub2>17? 17: sub2;
        pa = (const char*)((ln&16)? P.wu: P.wg) + ((size_t)(sub2*32 + 4*mt + ((ln>>2)&3))*9216 + f0*2 + ((ln&3)==3? 272: (ln&3)*128));
      } else if(j<WD_J0){
        int n = (j+1)*64 + ln; n = n>71? 71: n;
        pa = (const char*)x + ((size_t)mt*294912 + ft*9216 + n*128);
      } else if(j<WD_J0+3){
        int n = (j-WD_J0)*64 + ln; n = n>161? 161: n;
        pa = (const char*)P.wd + ((size_t)sk*1327104 + ((xcd&1)*32 + idx)*20736 + n*128);
      } else {
        int a = ln>>3;
        const u16* ar = a==0? P.Are : a==1? P.Aim : a==2? P.Bre : a==3? P.Bim : a==4? P.Cre : P.Cim;
        pa = (const char*)ar + ((is_ssm? sb: 0)*1024 + (ln&7)*128);
      }
      pf4(pa, lds0 + 156672 + w*256);
    };
    warm_code();
    #pragma unroll
    for(int sub=0;sub<3;sub++){
      #pragma unroll
      for(int i=0;i<4;i++) issue1(i,sub);
      if(w<2) issue1(4,sub);
      if(sub==2 && w>=4) junk(-1);
    }
    {
      unsigned long long e0;
      const int dch = (is_ssm? sb*4 : 0) + (w&3);
      unsigned ldw, dsw;
      asm volatile("s_load_dwordx2 %0, %3, 0x0\n\ts_load_dword %1, %4, 0x0\n\ts_load_dword %2, %5, 0x0\n\ts_waitcnt lgkmcnt(0)"
                   : "=&s"(e0), "=&s"(ldw), "=&s"(dsw) : "s"(ws + WS_E), "s"(P.ldt + (dch&~1)), "s"(P.Dsk + (dch&~1)) : "memory");
      tag = e0 + TAG_STEP;
      ld_ds = (dch&1)? ((ldw>>16) | (dsw&0xffff0000u)) : ((ldw&0xffffu) | (dsw<<16));
    }
    auto start = [&](auto LOW){
      #pragma unroll
      for(int i=0;i<9;i++)
      #pragma unroll
      for(int k=0;k<16;k++){ float z; asm volatile("v_mov_b32 %0, 0" : "=v"(z)); acc[i][k] = z; }
      if(decltype(LOW)::value && w<2) wait_vm<10>(); else wait_vm<8>();
      BAR();

      B0 = *(LAS bf16x8*)(smem + b_addr[0]);
      #pragma unroll
      for(int i=0;i<6;i++) rdA(A[i], smem, i);
      SB0();
    };
    auto issueWd = [&](int i, int s2){
      int j = (i<2)? (w + 8*i) : (16 + (w&1)); int c = j*64+lane; int row = c/18, col8 = c%18;
      glds16((const char*)P.wd + (size_t)s2*64*1152, (unsigned)((sk*1152 + row)*1152 + ((mem&3)*144 + col8*8)*2), lds0 + G2_RB + ((s2+3)%6)*G2_SUB + j*1024);
    };
    const __amdgpu_buffer_rsrc_t rsU = __builtin_amdgcn_make_buffer_rsrc((void*)(ws + WS_UT), 0, D*L*2, 0x00020000);
    auto put_uT = [&](LAS char* sb_){
      int ln = lane; asm volatile("" : "+v"(ln));
      const int g4 = ln>>4, rr = (ln&15)>>2, cc = ln&3;
      #pragma unroll
      for(int hf=0;hf<2;hf++){
        int r0 = 32*w + 8*g4 + rr, r1 = r0 + 4;
        int ch = 2*hf + (cc>>1);
        s16x4 lo = __builtin_amdgcn_ds_read_tr16_b64_v4i16((LAS s16x4*)(sb_ + r0*64 + ((ch ^ ((r0>>2)&3))*16) + (cc&1)*8));
        s16x4 hi = __builtin_amdgcn_ds_read_tr16_b64_v4i16((LAS s16x4*)(sb_ + r1*64 + ((ch ^ ((r1>>2)&3))*16) + (cc&1)*8));
        u32x4 v = __builtin_bit_cast(u32x4, __builtin_shufflevector(lo,hi,0,1,2,3,4,5,6,7));
        *(LAS u32x4*)(smem + G1_LDS + w*2048 + hf*1024 + ln*16) = v;
      }
    };
    auto flush_uT = [&](){
      int ln = lane; asm volatile("" : "+v"(ln));
      #pragma unroll
      for(int hf=0;hf<2;hf++){
        u32x4 v = *(LAS u32x4*)(smem + G1_LDS + w*2048 + hf*1024 + ln*16);
        __builtin_amdgcn_raw_buffer_store_b128(v, rsU, ((ft*32 + 16*hf + (ln&15))*L + m0 + 32*w + 8*(ln>>4))*2, 0, 16);
      }
    };
    auto body = [&](auto PH, int j, int mode){
      constexpr int ph = decltype(PH)::value;
      LAS char* cur = smem + (j&3)*G1_SUB;
      LAS char* nxt = smem + ((j+1)&3)*G1_SUB;
      if(j==ft && j<17) put_uT(cur);
      if(j==16 && ft==17) put_uT(nxt);
      if((j==15 && ft<16) || (j==16 && (ft==16 || ft==17))) flush_uT();
      SB0();
      #pragma unroll
      for(int i=0;i<18;i++){
        const int t = i%9;
        acc[t] = __builtin_amdgcn_mfma_f32_32x32x16_bf16(mkfrag(A[i%6]), (i<9)? B0 : B1, acc[t],0,0,0);
        if(i+6<18) rdA(A[i%6], cur, i+6); else rdA(A[i%6], nxt, i+6-18);
        if(i==2)  B1 = *(LAS bf16x8*)(cur + b_addr[1]);
        if(i==11) B0 = *(LAS bf16x8*)(nxt + b_addr[0]);
        if(mode==2){ if(i<16 && (i&1)==0){ int pc=i>>1; if(pc<6) issueWd(pc&1, pc>>1); } if(w<2 && (i==13||i==15||i==17)) issueWd(2,(i-13)>>1); }
        if(mode==2 && i==1 && is_ssm) ssm_load(sin, sb*4 + (w&3), w<4, P.Are,P.Aim,P.Bre,P.Bim,P.Cre,P.Cim);
        if(mode==1){
          constexpr int o = G1_STAG*ph;
          if(i==o+0) issue1(0,j+3);
          if(i==o+G1_GAP) issue1(1,j+3);
          if(i==o+2*G1_GAP) issue1(2,j+3);
          if(i==o+3*G1_GAP) issue1(3,j+3);
          if(ph==0 && i==4*G1_GAP && w<2) issue1(4,j+3);
          if(ph==1 && i==JUNK_I) junk(j);
        }
        SB0();
      }
    };
    if(w<4){
      start(IC(1));
      #pragma unroll 1
      for(int j=0;j<15;j++){
        if(w<2) wait_vm<5>(); else wait_vm<4>();
        BAR();

        body(IC(0),j,1);
      }
    } else {
      start(IC(0));
      #pragma unroll 1
      for(int j=0;j<15;j++){
        wait_vm<5>();
        BAR();
        body(IC(1),j,1);
      }
    }

    if(w<2 || w>=4) wait_vm<5>(); else wait_vm<4>();
    BAR();
    warm_code();
    body(IC(0),15,0);
    if(ft<16) wait_vm<2>(); else wait_vm<0>();
    BAR();
    body(IC(0),16,0);
    wait_vm<0>(); BAR();
    if(tid==0) __hip_atomic_store((gu64*)(ws + WS_F0) + bid*16, tag, RLX_AGENT);
    body(IC(0),17,2);
    BAR();

    if(is_ssm) ssm_load_idx(sin);
    if(!is_ssm && bid < SSM_WGS+64) gate_load(gin, bid-SSM_WGS, P.u, P.gw);
    SB0();
    const int tt16 = (m0>>4) + 2*w + ((lane>>4)&1);
    const __amdgpu_buffer_rsrc_t rsH = __builtin_amdgcn_make_buffer_rsrc((void*)hfrag, 0, L*F*2, 0x00020000);
    #pragma unroll
    for(int t=0;t<9;t++){
      int Ft = ft*9 + t; int fs = Ft>>1, half = Ft&1; int qq = 2*half + h;
      u32x4 v;
      v[0]=pk_bf16(acc[t][0]*acc[t][8],  acc[t][1]*acc[t][9]);
      v[1]=pk_bf16(acc[t][2]*acc[t][10], acc[t][3]*acc[t][11]);
      v[2]=pk_bf16(acc[t][4]*acc[t][12], acc[t][5]*acc[t][13]);
      v[3]=pk_bf16(acc[t][6]*acc[t][14], acc[t][7]*acc[t][15]);
      __builtin_amdgcn_raw_buffer_store_b128(v, rsH, ((tt16*144 + fs)*64 + 16*qq + (lane&15))*16, 0, 16);
    }

  }
  gu64* const F1 = (gu64*)(ws + WS_F1);
  gu64* const F2 = (gu64*)(ws + WS_F2);
  auto pub1 = [&](){ wait_vm<0>(); if(lane==0) __hip_atomic_store(F1 + bid*16 + w, tag, RLX_AGENT); };
  SsmSt sst;
  gu64* const f1p = F1 + (xcd + 8*((idx&3) + 4*(lane>>3)))*16 + (lane&7);
  unsigned long long f1v = 0;
  if(is_ssm){
    gu64* const f0p = (gu64*)(ws + WS_F0) + (((lane&7)>>2) + 2*(sb>>6) + 8*((lane&3) + 4*((sb>>3)&7)))*16;
    unsigned long long f0v;
    asm volatile("global_load_dwordx2 %0, %1, off sc1" : "=v"(f0v) : "v"(f0p) : "memory");
    ssm_part1(smem, sin, ld_ds&0xffffu, ld_ds>>16, sst, (const u16*)(ws + WS_UT) + (size_t)sb*4*L,
      [&](){ pub1();
             asm volatile("" : "+v"(f0v));
             if(w==0 && !__all(f0v==tag)) wait_tags(f0p, true, tag); },
      [&](){ asm volatile("global_load_dwordx2 %0, %1, off sc1" : "=v"(f1v) : "v"(f1p) : "memory"); },
      [&](){  });
    wait_vm<0>();
    asm volatile("" : "+v"(f1v));
  } else {
    if(bid < SSM_WGS+64) gate_phase(gin, bid-SSM_WGS, (float*)(ws+WS_G));
    pub1();

  }
  const int q4 = w&3, kh = w>>2;
  const int tm = 2*mt + (mem>>2), dn = mem&3;
  f32x4 acc[2][9];
  gu64* const F3 = (gu64*)(ws + WS_F3);
  gu64* f3p; unsigned long long f3v;
  {
    int pb;
    if(lane<36){ int s2 = dn*36 + lane; pb = s2/18 + 8*(s2%18); } else pb = SSM_WGS + tm*4 + (lane&3);
    f3p = F3 + pb*16; }
  {
    unsigned wsrc[3]; unsigned wdst[3];
    #pragma unroll
    for(int i=0;i<3;i++){
      int j = (i<2)? (w + 8*i) : (16 + (w&1)); int c = j*64+lane; int row = c/18, col8 = c%18;
      wsrc[i] = (sk*1152 + row)*1152 + (dn*144 + col8*8)*2; wdst[i] = j*1024;
    }
    auto issueW1 = [&](int i, int sub){
      glds16((const char*)P.wd + (size_t)sub*64*1152, wsrc[i], lds0 + G2_RB + ((sub+3)%G2_NB)*G2_SUB + wdst[i]);
    };
    const char* hbase = (const char*)hfrag + (size_t)((tm*8 + 2*q4)*144 + sk*36 + kh)*1024;
    const unsigned hoff0 = lane*16, hoff1 = lane*16 + 144*1024;
    bf16x8 hb[G2_RH][2];
    auto issueH = [&](int sub, bf16x8* dst){
      gload16(dst[0], hbase + (size_t)(sub*2)*1024, hoff0);
      gload16(dst[1], hbase + (size_t)(sub*2)*1024, hoff1);
    };
    if(w==0){
      if(!__all(f1v==tag)) wait_tags(f1p, true, tag);
    }
    LBAR();

    if(is_ssm){
      f32x4 yacc[8];
      LAS char* wv = smem + S_WAVE0 + (w&3)*SW_SIZE;
      auto hook = [&](int k){ if(k<2*G2_RH) gload16(hb[k>>1][k&1], hbase + (size_t)((k>>1)*2)*1024, (k&1)? hoff1 : hoff0); };
      if(w<4) ssm_conv<0>(wv, yacc, hook); else ssm_conv<1>(wv, yacc, hook);
      wait_vm<0>();
      #pragma unroll
      for(int s=0;s<G2_RH;s++) asm volatile("" : "+v"(hb[s][0]), "+v"(hb[s][1]));

      LBAR();
      ssm_out(wv, kh, sb*4 + (w&3), yacc, sst, (u16*)(ws+WS_AT));
    } else {
      #pragma unroll
      for(int s=0;s<G2_RH;s++) issueH(s, hb[s]);
      wait_vm<0>();
      #pragma unroll
      for(int s=0;s<G2_RH;s++) asm volatile("" : "+v"(hb[s][0]), "+v"(hb[s][1]));

    }

    BAR();
    #pragma unroll
    for(int s=3;s<6;s++){ issueW1(0,s); issueW1(1,s); if(w<2) issueW1(2,s); }

    const int qg = lane>>4, q=(lane&15)>>2, p=lane&3;
    const int a_base = (16*(qg>>1) + 4*(qg&1) + q)*288 + 8*p + kh*32*288;
    #pragma unroll
    for(int i=0;i<2;i++)
    #pragma unroll
    for(int t=0;t<9;t++) acc[i][t] = (f32x4){0,0,0,0};
    AFrag A[9];
    auto rdA = [&](AFrag& f, int sub, int t){
      LAS char* sb = smem + G2_RB + ((sub+3)%G2_NB)*G2_SUB;
      f.lo = __builtin_amdgcn_ds_read_tr16_b64_v4i16((LAS s16x4*)(sb + a_base + t*32));
      f.hi = __builtin_amdgcn_ds_read_tr16_b64_v4i16((LAS s16x4*)(sb + a_base + 8*288 + t*32));
    };
    #pragma unroll
    for(int t=0;t<9;t++) rdA(A[t], 0, t);
    SB0();
    auto body = [&](auto JJ){
      constexpr int j = decltype(JJ)::value;
      constexpr int N3 = g2_wait(j,3), N2 = g2_wait(j,2);
      if(N3==N2) wait_vm<N2>(); else { if(w<2) wait_vm<N3>(); else wait_vm<N2>(); }
      BAR();

      if(j==2 && tid==0) __hip_atomic_store(F3 + bid*16, tag, RLX_AGENT);
      if(j==14) asm volatile("global_load_dwordx2 %0, %1, off sc1" : "=v"(f3v) : "v"(f3p) : "memory");
      asm volatile("" : "+v"(hb[j%G2_RH][0]), "+v"(hb[j%G2_RH][1]));
      #pragma unroll
      for(int t=0;t<9;t++){
        bf16x8 Af = mkfrag(A[t]);
        acc[0][t] = __builtin_amdgcn_mfma_f32_16x16x32_bf16(Af, hb[j%G2_RH][0], acc[0][t],0,0,0);
        acc[1][t] = __builtin_amdgcn_mfma_f32_16x16x32_bf16(Af, hb[j%G2_RH][1], acc[1][t],0,0,0);
        if(j<17) rdA(A[t], j+1, t);
        if(j>=1 && j<=12){
          if(t==1) issueW1(0,j+5);
          if(t==4) issueW1(1,j+5);
          if(t==6 && w<2) issueW1(2,j+5);
        }
        if(j>=1 && j+G2_RH-1<=17 && t==8){ issueH(j+G2_RH-1, hb[(j-1)%G2_RH]); }
        SB0();
      }
    };
    body(IC(0)); body(IC(1)); body(IC(2)); body(IC(3)); body(IC(4)); body(IC(5));
    body(IC(6)); body(IC(7)); body(IC(8)); body(IC(9)); body(IC(10)); body(IC(11));
    body(IC(12)); body(IC(13)); body(IC(14)); body(IC(15)); body(IC(16)); body(IC(17));
  }
  BAR();

  {
    asm volatile("" : "+v"(f3v));
    if(!__all(f3v==tag)) wait_tags(f3p, true, tag);
    const int fr = lane&15, fq = lane>>4;
    const int tok = tm*128 + q4*32 + kh*16 + fr;
    const int t0 = (sk - w)&3;
    const __amdgpu_buffer_rsrc_t rsA = __builtin_amdgcn_make_buffer_rsrc((void*)(ws + WS_AT), 0, D*L*2, 0x00020000);
    const __amdgpu_buffer_rsrc_t rsG = __builtin_amdgcn_make_buffer_rsrc((void*)(ws + WS_G), 0, L*4, 0x00020000);
    const float gg = __uint_as_float(__builtin_amdgcn_raw_buffer_load_b32(rsG, tok*4, 0, 16));
    unsigned av[3][4];
    #pragma unroll
    for(int k=0;k<3;k++){
      const int t = (k<2)? t0+4*k : (t0==0? 8 : t0+4);
      #pragma unroll
      for(int r=0;r<4;r++) av[k][r] = __builtin_amdgcn_raw_buffer_load_b16(rsA, ((dn*144 + t*16 + fq*4 + r)*L + tok)*2, 0, 16);
    }
    SB0();
    LAS char* xw = smem + w*9216 + lane*16;
    LAS char* xr = smem + (w^4)*9216 + lane*16;
    #pragma unroll
    for(int t=0;t<9;t++) *(LAS f32x4*)(xw + t*1024) = kh? acc[0][t] : acc[1][t];
    __syncthreads();
    f32x4 s9[9];
    #pragma unroll
    for(int t=0;t<9;t++) s9[t] = (kh? acc[1][t] : acc[0][t]) + *(LAS f32x4*)(xr + t*1024);
    char* slab = ws + WS_P + (size_t)((tm*4+dn)*8 + w)*9*4*512;
    const __amdgpu_buffer_rsrc_t rsP = __builtin_amdgcn_make_buffer_rsrc((void*)slab, 0, 9*4*512, 0x00020000);
    LAS char* keep = smem + 73728 + w*3072 + lane*16;
    #pragma unroll
    for(int t=0;t<9;t++){
      if(((t + w)&3) != sk) __builtin_amdgcn_raw_buffer_store_b64((u32x2){pk_bf16(s9[t][0],s9[t][1]), pk_bf16(s9[t][2],s9[t][3])}, rsP, (t*4+sk)*512 + lane*8, 0, 16);
      else *(LAS f32x4*)(keep + (t>>2)*1024) = s9[t];
    }
    wait_vm<0>();
    BAR();
    if(tid==0) __hip_atomic_store(F2 + bid*16, tag, RLX_AGENT);

    if(w==0){
      const int pb = (xcd&1) + 2*((sk + 1 + (lane&1) + ((lane>>1)&1)*2)&3) + 8*idx;
      wait_tags(F2 + pb*16, lane<3, tag);
    }
    if(bid==0 && w==7){
      bool ok=false;
      for(unsigned spins=0; spins<kSpinCap && !ok; ++spins){
        bool m=true;
        #pragma unroll
        for(int i=0;i<4;i++) m &= (__hip_atomic_load(F1 + (lane+64*i)*16, RLX_AGENT)==tag);
        ok = __all(m);
      }
      if(ok && lane==0) __hip_atomic_store((gu64*)(ws+WS_E), tag, RLX_AGENT);
    }
    BAR();

    u32x2 pl[3][3];
    #pragma unroll
    for(int k=0;k<3;k++){
      const int t = (k<2)? t0+4*k : (t0==0? 8 : t0+4);
      #pragma unroll
      for(int s=0;s<3;s++) pl[k][s] = __builtin_amdgcn_raw_buffer_load_b64(rsP, (t*4 + ((sk+1+s)&3))*512 + lane*8, 0, 16);
    }
    const float og = rbf(1.f-gg);
    #pragma unroll
    for(int k=0;k<3;k++){
      const int t = t0+4*k;
      if(k<2 || t0==0){
        f32x4 s = *(LAS f32x4*)(keep + k*1024);
        #pragma unroll
        for(int q2=0;q2<3;q2++){ u32x2 pv = pl[k][q2];
          s[0]+=__uint_as_float(pv[0]<<16); s[1]+=__uint_as_float(pv[0]&0xffff0000u); s[2]+=__uint_as_float(pv[1]<<16); s[3]+=__uint_as_float(pv[1]&0xffff0000u); }
        float o[4];
        #pragma unroll
        for(int r=0;r<4;r++) o[r] = rbf(gg*bf2f((u16)av[k][r])) + rbf(og*rbf(s[r]));
        *(uint2*)(P.out + tok*D + dn*144 + t*16 + fq*4) = make_uint2(pk_bf16(o[0],o[1]), pk_bf16(o[2],o[3]));
      }
    }
  }

}

extern "C" void kernel_launch(void* const* d_in, const int* in_sizes, int n_in, void* d_out, int out_size, void* d_ws, size_t ws_size, hipStream_t stream) {
  Params P;
  P.u=(const u16*)d_in[0]; P.Are=(const u16*)d_in[1]; P.Aim=(const u16*)d_in[2]; P.Bre=(const u16*)d_in[3]; P.Bim=(const u16*)d_in[4];
  P.Cre=(const u16*)d_in[5]; P.Cim=(const u16*)d_in[6]; P.ldt=(const u16*)d_in[7]; P.Dsk=(const u16*)d_in[8];
  P.wg=(const u16*)d_in[10]; P.wu=(const u16*)d_in[11]; P.wd=(const u16*)d_in[12]; P.gw=(const u16*)d_in[13];
  P.out=(u16*)d_out; P.ws=(char*)d_ws + ((ws_size - ((size_t)44<<20)) & ~(((size_t)2<<20)-1));
  (void)hipFuncSetAttribute((const void*)k_fused, hipFuncAttributeMaxDynamicSharedMemorySize, FUSED_LDS);
  k_fused<<<256,512,FUSED_LDS,stream>>>(P);
}
```
